```python
import torch

def _softmax(x, axis=-1):
    return torch.softmax(x, dim=axis)
def _gelu(x):
    return torch.nn.functional.gelu(x, approximate="tanh")
def _silu(x):
    return torch.nn.functional.silu(x)
def _sigmoid(x):
    return torch.sigmoid(x)
def _layernorm(x, axis=-1, eps=1e-5):
    m = x.mean(dim=axis, keepdim=True)
    v = x.var(dim=axis, keepdim=True, unbiased=False)
    return (x - m) / torch.sqrt(v + eps)
def _scatter_add(tgt, idx, src, axis=0):
    return tgt.clone().index_add_(axis, idx, src)
def _segment_sum(src, seg, n):
    out = torch.zeros((n,) + src.shape[1:], dtype=src.dtype)
    return out.index_add_(0, seg, src)
def _pad(x, pw):
    flat = tuple(v for pair in reversed(pw) for v in pair)
    return torch.nn.functional.pad(x, flat)

kernel_name = 'sequential_preLN__linear_kernel_hedgehog__mixer_channel_mlp__prompt'

def setup_inputs():

    _w = (lambda a: (lambda t: t.to(torch.bfloat16) if t.is_floating_point() else t)(torch.as_tensor(a)))
    def _A_setup():
        B, S, D, H = 1, 2048, 448, 16
        Dh = D // H
        return {
            "x": _w((lambda *s: torch.randn(*s, dtype=torch.bfloat16) * 0.05)(B, S, D)),
            "wqkv": _w((lambda *s: torch.randn(*s, dtype=torch.bfloat16) * 0.2)(D, 3 * D)),
            "w_phi": _w((lambda *s: torch.randn(*s, dtype=torch.bfloat16) * 0.2)(H, Dh, Dh) * (Dh ** -0.5)),
            "wo": _w((lambda *s: torch.randn(*s, dtype=torch.bfloat16) * 0.05)(D, D)),
        }
    _A_in = _A_setup()

    def _B_setup():
        B, S, D, F = 1, 2048, 448, 1792
        return {
            "x": _w((lambda *s: torch.randn(*s, dtype=torch.bfloat16) * 0.05)(B, S, D)),
            "w1": _w((lambda *s: torch.randn(*s, dtype=torch.bfloat16) * 3.2)(D, F) * (D ** -0.5)),
            "b1": _w((lambda *s: torch.randn(*s, dtype=torch.bfloat16) * 0.8)(F)),
            "w2": _w((lambda *s: torch.randn(*s, dtype=torch.bfloat16) * 0.1)(F, D) * (F ** -0.5)),
            "b2": _w((lambda *s: torch.randn(*s, dtype=torch.bfloat16) * 0.1)(D)),
        }
    _B_in = _B_setup()

    return {
        "A_x": _A_in["x"],
        "A_wqkv": _A_in["wqkv"],
        "A_w_phi": _A_in["w_phi"],
        "A_wo": _A_in["wo"],
        "B_x": _B_in["x"],
        "B_w1": _B_in["w1"],
        "B_b1": _B_in["b1"],
        "B_w2": _B_in["w2"],
        "B_b2": _B_in["b2"],
    }

def reference(A_x, A_wqkv, A_w_phi, A_wo, B_x, B_w1, B_b1, B_w2, B_b2):
    def _A_ref(x, wqkv, w_phi, wo):
        B, S, D = x.shape
        H = 16; Dh = D // H
        qkv = (lambda x, *d: x.permute(*d))((x @ wqkv).reshape(B, S, 3, H, Dh), 2, 0, 3, 1, 4)
        q, k, v = qkv[0], qkv[1], qkv[2]
        pq = torch.einsum("bhsd,hdf->bhsf", q, w_phi)
        pk = torch.einsum("bhsd,hdf->bhsf", k, w_phi)
        phi_q = torch.concatenate([_softmax(pq, axis=-1), _softmax(-pq, axis=-1)], axis=-1)
        phi_k = torch.concatenate([_softmax(pk, axis=-1), _softmax(-pk, axis=-1)], axis=-1)
        kv = torch.einsum("bhsf,bhsd->bhfd", phi_k, v)
        z  = torch.einsum("bhsf->bhf", phi_k)
        num = torch.einsum("bhqf,bhfd->bhqd", phi_q, kv)
        den = torch.einsum("bhqf,bhf->bhq", phi_q, z)[..., None] + 1e-6
        o = num / den
        return (lambda x, *d: x.permute(*d))(o, 0, 2, 1, 3).reshape(B, S, D) @ wo

    def _B_ref(x, w1, b1, w2, b2):
        y = _layernorm(x, axis=-1)
        h = _gelu(torch.einsum("bsd,df->bsf", y, w1) + b1)
        return torch.einsum("bsf,fd->bsd", h, w2) + b2

    x = A_x
    h = x + _A_ref(_layernorm(x, axis=-1), A_wqkv, A_w_phi, A_wo)
    h2 = h + _B_ref(_layernorm(h, axis=-1), B_w1, B_b1, B_w2, B_b2)
    return h2
```

```cpp
#include <hip/hip_runtime.h>
#include <hip/hip_bf16.h>
typedef __hip_bfloat16 bf16;
constexpr int S=2048, D=448, H=16, DH=28, F=1792;
__device__ inline float b2f(bf16 v){ return __bfloat162float(v);}
__device__ inline float rb(float v){ return __bfloat162float(__float2bfloat16(v)); }

__global__ void ln_kernel(const bf16* x, float* y, int twice){
  int r = blockIdx.x; __shared__ float sh[D]; __shared__ float red[2];
  for (int i=threadIdx.x;i<D;i+=blockDim.x) sh[i]=b2f(x[r*D+i]);
  __syncthreads();
  for (int t=0;t<=twice;t++){
    if (threadIdx.x==0){ float m=0; for(int i=0;i<D;i++) m+=sh[i]; m/=D; float v=0; for(int i=0;i<D;i++) v+=(sh[i]-m)*(sh[i]-m); v/=D; red[0]=m; red[1]=rsqrtf(v+1e-5f);}
    __syncthreads();
    for (int i=threadIdx.x;i<D;i+=blockDim.x) sh[i]=rb((sh[i]-red[0])*red[1]);
    __syncthreads();
  }
  for (int i=threadIdx.x;i<D;i+=blockDim.x) y[r*D+i]=sh[i];
}
__global__ void gemm_kernel(const float* A, const bf16* W, float* C, int M,int N,int K){
  int n = blockIdx.x*blockDim.x+threadIdx.x; int m = blockIdx.y; if(n>=N) return;
  float acc=0; for(int k=0;k<K;k++) acc+=A[m*K+k]*b2f(W[k*N+n]);
  C[m*N+n]=rb(acc);
}
__global__ void phi_kernel(const float* qkv, const bf16* wphi, float* phi  ){
  int s=blockIdx.x; int t=threadIdx.x; if(t>=2*H) return; int which=t/H, h=t%H;
  float p[DH];
  for(int f=0;f<DH;f++){ float a=0; for(int d=0;d<DH;d++) a+=qkv[s*3*D+which*D+h*DH+d]*b2f(wphi[h*DH*DH+d*DH+f]); p[f]=rb(a);}
  float mx=-1e30f,mn=1e30f; for(int f=0;f<DH;f++){mx=fmaxf(mx,p[f]);mn=fminf(mn,p[f]);}
  float s1=0,s2=0; for(int f=0;f<DH;f++){s1+=expf(p[f]-mx); s2+=expf(mn-p[f]);}
  float* o=phi+(((size_t)which*S+s)*H+h)*56;
  for(int f=0;f<DH;f++){o[f]=rb(expf(p[f]-mx)/s1); o[DH+f]=rb(expf(mn-p[f])/s2);}
}
__global__ void kv_kernel(const float* qkv,const float* phi,float* kv  ){
  int h=blockIdx.x, f=blockIdx.y, d=threadIdx.x; if(d>DH) return; float a=0;
  for(int s=0;s<S;s++){ float pk=phi[(((size_t)1*S+s)*H+h)*56+f]; a+= d<DH? pk*qkv[s*3*D+2*D+h*DH+d] : pk; }
  kv[(h*56+f)*29+d]=rb(a);
}
__global__ void o_kernel(const float* phi,const float* kv,float* o){
  int s=blockIdx.x; int t=threadIdx.x; if(t>=D) return; int h=t/DH,d=t%DH; float n=0,z=0;
  for(int f=0;f<56;f++){ float pq=phi[(((size_t)0*S+s)*H+h)*56+f]; n+=pq*kv[(h*56+f)*29+d]; z+=pq*kv[(h*56+f)*29+28]; }
  o[s*D+t]=rb(rb(n)/rb(rb(z)+1e-6f));
}
__global__ void add_kernel(const bf16* x,const float* a, bf16* h){ int i=blockIdx.x*blockDim.x+threadIdx.x; if(i<S*D) h[i]=__float2bfloat16(b2f(x[i])+a[i]); }
__global__ void gelu_kernel(float* a,const bf16* b1){ int i=blockIdx.x*blockDim.x+threadIdx.x; if(i<S*F){ float v=rb(a[i]+b2f(b1[i%F])); float t=tanhf(0.7978845608f*(v+0.044715f*v*v*v)); a[i]=rb(0.5f*v*(1+t)); } }
__global__ void fin_kernel(const bf16* h,const float* a,const bf16* b2,bf16* out){ int i=blockIdx.x*blockDim.x+threadIdx.x; if(i<S*D) out[i]=__float2bfloat16(b2f(h[i])+rb(a[i]+b2f(b2[i%D]))); }

extern "C" void kernel_launch(void* const* d_in, const int* in_sizes, int n_in, void* d_out, int out_size, void* d_ws, size_t ws_size, hipStream_t stream){
  const bf16* x=(const bf16*)d_in[0]; const bf16* wqkv=(const bf16*)d_in[1]; const bf16* wphi=(const bf16*)d_in[2]; const bf16* wo=(const bf16*)d_in[3];
  const bf16* w1=(const bf16*)d_in[5]; const bf16* b1=(const bf16*)d_in[6]; const bf16* w2=(const bf16*)d_in[7]; const bf16* b2=(const bf16*)d_in[8];
  float* ws=(float*)d_ws; float* xn=ws; float* qkv=xn+S*D; float* phi=qkv+S*3*D; float* kv=phi+2*S*H*56; float* o=kv+H*56*32; float* ao=o+S*D; float* y=ao+S*D; float* hh=y+S*D; float* m2=hh+S*F; bf16* h=(bf16*)(m2+S*D);
  ln_kernel<<<S,64,0,stream>>>(x,xn,0);
  gemm_kernel<<<dim3((3*D+63)/64,S),64,0,stream>>>(xn,wqkv,qkv,S,3*D,D);
  phi_kernel<<<S,64,0,stream>>>(qkv,wphi,phi);
  kv_kernel<<<dim3(H,56),64,0,stream>>>(qkv,phi,kv);
  o_kernel<<<S,448,0,stream>>>(phi,kv,o);
  gemm_kernel<<<dim3(D/64,S),64,0,stream>>>(o,wo,ao,S,D,D);
  add_kernel<<<S*D/256,256,0,stream>>>(x,ao,h);
  ln_kernel<<<S,64,0,stream>>>(h,y,1);
  gemm_kernel<<<dim3(F/64,S),64,0,stream>>>(y,w1,hh,S,F,D);
  gelu_kernel<<<S*F/256,256,0,stream>>>(hh,b1);
  gemm_kernel<<<dim3(D/64,S),64,0,stream>>>(hh,w2,m2,S,D,F);
  fin_kernel<<<S*D/256,256,0,stream>>>(h,m2,b2,(bf16*)d_out);
}
```

```cpp
#include <hip/hip_runtime.h>
#include <hip/hip_bf16.h>

typedef unsigned short u16;
typedef unsigned long long u64;
typedef __bf16 bf16x2 __attribute__((ext_vector_type(2)));
typedef __bf16 bf16x4 __attribute__((ext_vector_type(4)));
typedef __bf16 bf16x8 __attribute__((ext_vector_type(8)));
typedef float f32x2 __attribute__((ext_vector_type(2)));
typedef float f32x4 __attribute__((ext_vector_type(4)));
typedef unsigned u32x2 __attribute__((ext_vector_type(2)));
typedef unsigned u32x4 __attribute__((ext_vector_type(4)));
typedef __amdgpu_buffer_rsrc_t rsrc_t;
#define LAS __attribute__((address_space(3)))
#define GAS __attribute__((address_space(1)))
#define CAS __attribute__((address_space(4)))
#define DI __device__ __forceinline__
#define UNR _Pragma("unroll")

constexpr unsigned WS_FA1 = 4096, WS_FA2 = 8192, WS_FB = 12288;
constexpr unsigned WS_KV = 65536;
constexpr unsigned WS_O = WS_KV + (1u << 20);
constexpr unsigned WS_G = WS_O + (2u << 20);
constexpr unsigned G_H = 0, G_ST = 114688, G_HH = 118784, G_SZ = 589824;
constexpr unsigned WS_END = WS_G + 16 * G_SZ;
constexpr unsigned WS_DBG = 16u << 20;
constexpr unsigned L_WK = 0, L_WV = 28672, L_WQ = 57344, L_WO1 = 86016, L_PV = 130048, L_KVF = 158720, L_CS = 162816;
constexpr unsigned L_W1 = 0, L_OST = 65536, L_STG = 100352, LDS_BYTES = 163840;
constexpr unsigned SENT = 0xFFFFFFFFu;
constexpr int SPIN_CAP = 1 << 19;

DI unsigned rfl(unsigned v) { return __builtin_amdgcn_readfirstlane(v); }
DI rsrc_t mkrsrc(const void* p, unsigned bytes) { return __builtin_amdgcn_make_buffer_rsrc((void*)p, (short)0, (int)bytes, 0x00020000); }
DI bf16x4 trd(unsigned a) { return __builtin_amdgcn_ds_read_tr16_b64_v4bf16((LAS bf16x4*)a); }
DI bf16x8 cat(bf16x4 a, bf16x4 b) { return __builtin_shufflevector(a, b, 0, 1, 2, 3, 4, 5, 6, 7); }
DI bf16x8 trfrag(unsigned a0, unsigned a1) { return cat(trd(a0), trd(a1)); }
DI f32x4 mfma(bf16x8 a, bf16x8 b, f32x4 c) { return __builtin_amdgcn_mfma_f32_16x16x32_bf16(a, b, c, 0, 0, 0); }
DI unsigned pk2(float a, float b) { f32x2 f = {a, b}; return __builtin_bit_cast(unsigned, __builtin_convertvector(f, bf16x2)); }
DI bf16x8 asb(u32x4 v) { return __builtin_bit_cast(bf16x8, v); }
DI float blo(unsigned u) { return __builtin_bit_cast(float, u << 16); }
DI float bhi(unsigned u) { return __builtin_bit_cast(float, u & 0xffff0000u); }
DI unsigned f2u(float f) { return __builtin_bit_cast(unsigned, f); }
DI float u2f(unsigned u) { return __builtin_bit_cast(float, u); }
DI float allq_sum(float v) {
  auto a = __builtin_amdgcn_permlane32_swap(f2u(v), f2u(v), false, false); float t = u2f(a[0]) + u2f(a[1]);
  auto b = __builtin_amdgcn_permlane16_swap(f2u(t), f2u(t), false, false); return u2f(b[0]) + u2f(b[1]);
}
DI float allq_max(float v) {
  auto a = __builtin_amdgcn_permlane32_swap(f2u(v), f2u(v), false, false); float t = fmaxf(u2f(a[0]), u2f(a[1]));
  auto b = __builtin_amdgcn_permlane16_swap(f2u(t), f2u(t), false, false); return fmaxf(u2f(b[0]), u2f(b[1]));
}
DI float bcast_q3(float v) {
  auto a = __builtin_amdgcn_permlane32_swap(f2u(v), f2u(v), false, false);
  auto b = __builtin_amdgcn_permlane16_swap(a[1], a[1], false, false); return u2f(b[1]);
}
DI unsigned umax3(unsigned a, unsigned b, unsigned c) { return max(max(a, b), c); }
DI unsigned vmax(u32x4 v, unsigned m) { return umax3(umax3(v.x, v.y, v.z), v.w, m); }
DI bool anybad(unsigned m) { return __builtin_amdgcn_ballot_w64(m == SENT) != 0; }
#define WAITV(n) asm volatile("s_waitcnt vmcnt(%0)" :: "i"(n) : "memory")
#define BARRIER() asm volatile("s_waitcnt lgkmcnt(0)\n\ts_barrier" ::: "memory")
#define MEMFENCE() asm volatile("" ::: "memory")
DI void glds(unsigned ldsa, unsigned voff, rsrc_t rs, unsigned soff) {
  asm volatile("s_mov_b32 m0, %0\n\ts_nop 0\n\tbuffer_load_dwordx4 %1, %2, %3 offen lds" :: "s"(ldsa), "v"(voff), "s"(rs), "s"(soff) : "memory");
}
DI void glds_nt(unsigned ldsa, unsigned voff, rsrc_t rs, unsigned soff) {
  asm volatile("s_mov_b32 m0, %0\n\ts_nop 0\n\tbuffer_load_dwordx4 %1, %2, %3 offen nt lds" :: "s"(ldsa), "v"(voff), "s"(rs), "s"(soff) : "memory");
}
template <unsigned RS> DI unsigned img_voff(unsigned i, unsigned lane) { unsigned n = i * 64 + lane, row = (n * 9363u) >> 17; return row * RS + (n - row * 14u) * 16u; }
DI u32x4 ldsc1(rsrc_t rs, unsigned voff, unsigned soff) { return __builtin_amdgcn_raw_buffer_load_b128(rs, voff, soff, 16); }
DI void stsc1(u32x4 v, rsrc_t rs, unsigned voff, unsigned soff) { __builtin_amdgcn_raw_buffer_store_b128(v, rs, voff, soff, 16); }
DI void stsc1_2(u32x2 v, rsrc_t rs, unsigned voff, unsigned soff) { __builtin_amdgcn_raw_buffer_store_b64(v, rs, voff, soff, 16); }
DI f32x4 ldsf4(unsigned a) { return *(LAS f32x4*)a; }
DI u32x4 ldsu4(unsigned a) { return *(LAS u32x4*)a; }

#define NSTAMP 16
#define STAMP(k) do {} while (0)
#define CSTAMP(k) do {} while (0)

DI void featmap(const f32x4 a0, const f32x4 a1, const bf16x8 ph0, const bf16x8 ph1, const bool pad, float (&ep)[8], float (&en)[8]) {
  u32x4 kb = {pk2(a0[0], a0[1]), pk2(a0[2], a0[3]), pad ? 0u : pk2(a1[0], a1[1]), pad ? 0u : pk2(a1[2], a1[3])};
  const f32x4 z = {0.f, 0.f, 0.f, 0.f};
  f32x4 p0 = mfma(ph0, asb(kb), z), p1 = mfma(ph1, asb(kb), z);
  float p[8];
  UNR for (int i = 0; i < 4; i++) { p[i] = p0[i]; p[4 + i] = pad ? p0[i] : p1[i]; }
  float mx = fmaxf(fmaxf(fmaxf(p[0], p[1]), fmaxf(p[2], p[3])), fmaxf(fmaxf(p[4], p[5]), fmaxf(p[6], p[7])));
  float mn = fminf(fminf(fminf(p[0], p[1]), fminf(p[2], p[3])), fminf(fminf(p[4], p[5]), fminf(p[6], p[7])));
  mx = allq_max(mx); mn = -allq_max(-mn);
  const float L2E = 1.4426950408889634f;
  const float cx = -mx * L2E, cn = mn * L2E;
  float sp = 0.f, sn = 0.f;
  UNR for (int i = 0; i < 8; i++) {
    float e1 = __builtin_amdgcn_exp2f(fmaf(p[i], L2E, cx)), e2 = __builtin_amdgcn_exp2f(fmaf(p[i], -L2E, cn));
    if (i >= 4) { e1 = pad ? 0.f : e1; e2 = pad ? 0.f : e2; }
    ep[i] = e1; en[i] = e2; sp += e1; sn += e2;
  }
  sp = __builtin_amdgcn_rcpf(allq_sum(sp)); sn = __builtin_amdgcn_rcpf(allq_sum(sn));
  UNR for (int i = 0; i < 8; i++) { ep[i] *= sp; en[i] *= sn; }
}

__global__ void __launch_bounds__(512, 2) fused_kernel(const u16* __restrict__ x, const u16* __restrict__ wqkv, const u16* __restrict__ wphi, const u16* __restrict__ wo,
                                                       const u16* __restrict__ w1, const u16* __restrict__ b1, const u16* __restrict__ w2, const u16* __restrict__ b2,
                                                       u16* __restrict__ out, char* __restrict__ ws) {
  const unsigned tid = threadIdx.x, lane = tid & 63, wv = rfl(tid >> 6);
  const unsigned q = lane >> 4, r = lane & 15, qq = (lane >> 2) & 3, p = lane & 3;
  const unsigned w = blockIdx.x, x8 = w & 7, ii = w >> 3;
  const unsigned t1 = 4 * (x8 >> 1) + (ii >> 3), hd = 8 * (x8 & 1) + (ii & 7);
  const unsigned t2 = 2 * x8 + (ii >> 4), j = ii & 15, s = j >> 2, c = j & 3;
  const bool pad = (q == 3);
  u64 tag = *(const CAS u64*)ws + 1ull;
  asm volatile("" : "+s"(tag));
  const rsrc_t rX = mkrsrc(x, 2048 * 896), rW = mkrsrc(wqkv, 448 * 2688), rP = mkrsrc(wphi, 16 * 1568), rWO = mkrsrc(wo, 448 * 896);
  const rsrc_t rW1 = mkrsrc(w1, 448 * 3584), rW2 = mkrsrc(w2, 1792 * 896), rS = mkrsrc(ws, WS_END);
  const unsigned gbase = WS_G + t2 * G_SZ;
  const u32x4 sent4 = {SENT, SENT, SENT, SENT};
  const f32x4 zero4 = {0.f, 0.f, 0.f, 0.f};
  const u32x4 ones_u = {0x3f803f80u, 0x3f803f80u, 0x3f803f80u, 0x3f803f80u};
  const bf16x8 ones = asb(ones_u);
  int spins = 0;
  STAMP(0); CSTAMP(30);

  const unsigned kvslot = WS_KV + (hd * 16 + t1) * 4096 + wv * 512, oslot = WS_O + (((t1 * 4 + (wv >> 1)) * 16 + hd) * 2 + (wv & 1)) * 1024;
  stsc1_2(u32x2{SENT, SENT}, rS, lane * 8, kvslot);
  stsc1(sent4, rS, lane * 16, oslot);
  if (wv < 2) glds(L_KVF + wv * 1024, lane * 16, rP, hd * 1568 + wv * 1024);
  const unsigned wvoff = (lane >> 2) * 2688 + (((lane & 3) ^ ((lane >> 5) << 1)) << 4), wwin = (56 * hd) & ~15u;
  UNR for (int m = 0; m < 7; m++) { const unsigned n = wv + 8 * m, sec = n >= 28; glds(n * 1024, wvoff, rW, (n - 28 * sec) * (16 * 2688) + (sec + 1) * 896 + wwin); }
  u32x4 xf[14];
  { const unsigned xv = (128 * t1 + 16 * wv + r) * 896 + q * 16;
    UNR for (int ks = 0; ks < 14; ks++) asm volatile("buffer_load_dwordx4 %0, %1, %2, 0 offen offset:%3" : "=v"(xf[ks]) : "v"(xv), "s"(rX), "i"(ks * 64)); }
  UNR for (int m = 0; m < 4; m++) { const unsigned n = min(wv + 8 * m, 27u); glds(L_WQ + n * 1024, wvoff, rW, n * (16 * 2688) + wwin); }
  UNR for (int m = 0; m < 5; m++) { const unsigned n = wv + 8 * m; stsc1(sent4, rS, lane * 16, n < 36 ? gbase + (j * 36 + n) * 1024 : 0xF0000000u); }
  UNR for (int m = 0; m < 6; m++) { const unsigned n = wv + 8 * m; if (m < 5 || n < 43) glds(L_WO1 + n * 1024, img_voff<896>(n, lane), rWO, 224 * c); }
  constexpr int NY = 14;
  WAITV(14 + NY);
  BARRIER();
  STAMP(1);
  if (wv == 7 && lane < 2) __hip_atomic_store((GAS u64*)(ws + (lane ? WS_FA2 + (t1 * 16 + hd) * 8 : WS_FA1 + (hd * 16 + t1) * 8)), tag, __ATOMIC_RELAXED, __HIP_MEMORY_SCOPE_AGENT);
  bf16x8 ph0, ph1;
  { const unsigned r0 = (4 * q + qq) * 56, r1 = (pad ? 4 * q + qq : 16 + 4 * q + qq) * 56;
    ph0 = trfrag(L_KVF + r0 + 8 * p, L_KVF + r1 + 8 * p); ph1 = trfrag(L_KVF + r0 + 32 + 8 * p, L_KVF + r1 + 32 + 8 * p); }
  const unsigned sh = 8 * (hd & 1), wrow = (8 * q + qq) * 64;
  const unsigned aW0 = wrow + ((8 * p + sh) ^ ((q & 1) << 5)), aW1 = wrow + (min(32 + 8 * p + sh, 56u) ^ ((q & 1) << 5));
  if (wv < 4) {
    const unsigned a = ((wv & 1) ? aW1 : aW0) + (wv >> 1) * 28672; f32x4 cs = zero4;
    UNR for (int ks = 0; ks < 14; ks++) cs = mfma(trfrag(a + ks * 2048, a + ks * 2048 + 256), ones, cs);
    if (r == 0) *(LAS f32x4*)(L_CS + wv * 64 + q * 16) = cs;
  }
  f32x4 aK0 = zero4, aK1 = zero4, aV0 = zero4, aV1 = zero4, aS = zero4, aQ = zero4;
  UNR for (int ks = 0; ks < 14; ks++) {
    asm volatile("s_waitcnt vmcnt(%1)" : "+v"(xf[ks]) : "i"(13 - ks + NY));
    const bf16x8 xb = asb(xf[ks]);
    aK0 = mfma(trfrag(aW0 + L_WK + ks * 2048, aW0 + L_WK + ks * 2048 + 256), xb, aK0);
    aK1 = mfma(trfrag(aW1 + L_WK + ks * 2048, aW1 + L_WK + ks * 2048 + 256), xb, aK1);
    aV0 = mfma(trfrag(aW0 + L_WV + ks * 2048, aW0 + L_WV + ks * 2048 + 256), xb, aV0);
    aV1 = mfma(trfrag(aW1 + L_WV + ks * 2048, aW1 + L_WV + ks * 2048 + 256), xb, aV1);
    aS = mfma(ones, xb, aS); aQ = mfma(xb, xb, aQ);
  }
  float mean, rstd;
  { const unsigned ri = r & 3; float d = ri == 0 ? aQ[0] : ri == 1 ? aQ[1] : ri == 2 ? aQ[2] : aQ[3]; d = (q == (r >> 2)) ? d : 0.f;
    const float sq = allq_sum(d); mean = aS[0] * (1.f / 448.f); const float var = fmaxf(sq * (1.f / 448.f) - mean * mean, 0.f); rstd = __builtin_amdgcn_rsqf(var + 1e-5f); }
  WAITV(10);
  BARRIER();
  STAMP(2);
  { const float nm = -mean * rstd;
    const f32x4 c0 = ldsf4(L_CS + q * 16), c1 = ldsf4(L_CS + 64 + q * 16), c2 = ldsf4(L_CS + 128 + q * 16), c3 = ldsf4(L_CS + 192 + q * 16);
    UNR for (int i = 0; i < 4; i++) { aK0[i] = fmaf(aK0[i], rstd, nm * c0[i]); aK1[i] = fmaf(aK1[i], rstd, nm * c1[i]); aV0[i] = fmaf(aV0[i], rstd, nm * c2[i]); aV1[i] = fmaf(aV1[i], rstd, nm * c3[i]); }
    float ep[8], en[8];
    featmap(aK0, aK1, ph0, ph1, pad, ep, en);
    const unsigned a = L_PV + (16 * wv + r) * 224 + ((q ^ (r >> 2)) << 3);
    *(LAS u32x2*)(a) = u32x2{pk2(ep[0], ep[1]), pk2(ep[2], ep[3])};
    *(LAS u32x2*)(a + 32) = u32x2{pk2(ep[4], ep[5]), pk2(ep[6], ep[7])};
    *(LAS u32x2*)(a + 64) = u32x2{pk2(en[0], en[1]), pk2(en[2], en[3])};
    *(LAS u32x2*)(a + 96) = u32x2{pk2(en[4], en[5]), pk2(en[6], en[7])};
    *(LAS u32x2*)(a + 128) = u32x2{pk2(aV0[0], aV0[1]), pk2(aV0[2], aV0[3])};
    *(LAS u32x2*)(a + 160) = u32x2{pad ? 0x00003f80u : pk2(aV1[0], aV1[1]), pad ? 0u : pk2(aV1[2], aV1[3])};
  }
  BARRIER();
  { const unsigned ab = L_PV + (4 * q + qq) * 224 + ((p ^ q) << 3), aA = ab + (2 * (wv >> 2) + (wv & 1)) * 32, aB = ab + (4 + ((wv >> 1) & 1)) * 32;
    f32x4 kv = zero4;
    UNR for (int kk = 0; kk < 4; kk++) kv = mfma(trfrag(aA + kk * 7168, aA + kk * 7168 + 3584), trfrag(aB + kk * 7168, aB + kk * 7168 + 3584), kv);
    stsc1_2(u32x2{pk2(kv[0], kv[1]), pk2(kv[2], kv[3])}, rS, lane * 8, kvslot);
  }
  STAMP(3);
  UNR for (int m = 0; m < 7; m++) { const unsigned n = wv + 8 * m; glds(n * 1024, img_voff<896>(n, lane), rWO, 196 * 896 + 224 * c); }
  bf16x8 phq0, phq1;
  { f32x4 aQ0 = zero4, aQ1 = zero4;
    const unsigned a0 = aW0 + L_WQ, a1 = aW1 + L_WQ;
    if (wv >= 6) { const unsigned a = (wv & 1) ? a1 : a0; f32x4 cs = zero4;
      UNR for (int ks = 0; ks < 14; ks++) cs = mfma(trfrag(a + ks * 2048, a + ks * 2048 + 256), ones, cs);
      if (r == 0) *(LAS f32x4*)(L_CS + 256 + (wv & 1) * 64 + q * 16) = cs; }
    UNR for (int ks = 0; ks < 14; ks++) { const bf16x8 xb = asb(xf[ks]);
      aQ0 = mfma(trfrag(a0 + ks * 2048, a0 + ks * 2048 + 256), xb, aQ0); aQ1 = mfma(trfrag(a1 + ks * 2048, a1 + ks * 2048 + 256), xb, aQ1); }
    BARRIER();
    const float nm = -mean * rstd; const f32x4 c0 = ldsf4(L_CS + 256 + q * 16), c1 = ldsf4(L_CS + 320 + q * 16);
    UNR for (int i = 0; i < 4; i++) { aQ0[i] = fmaf(aQ0[i], rstd, nm * c0[i]); aQ1[i] = fmaf(aQ1[i], rstd, nm * c1[i]); }
    float ep[8], en[8];
    featmap(aQ0, aQ1, ph0, ph1, pad, ep, en);
    phq0 = asb(u32x4{pk2(ep[0], ep[1]), pk2(ep[2], ep[3]), pk2(ep[4], ep[5]), pk2(ep[6], ep[7])});
    phq1 = asb(u32x4{pk2(en[0], en[1]), pk2(en[2], en[3]), pk2(en[4], en[5]), pk2(en[6], en[7])});
  }
  if (wv == 7) {
    const unsigned fo = lane < 16 ? WS_FA1 + (hd * 16 + lane) * 8 : WS_FA2 + (t2 * 16 + (lane & 15)) * 8;
    for (;;) { MEMFENCE(); const u64 f = __hip_atomic_load((GAS u64*)(ws + fo), __ATOMIC_RELAXED, __HIP_MEMORY_SCOPE_AGENT);
      if (__builtin_amdgcn_ballot_w64(f != tag) == 0 || ++spins > SPIN_CAP) break; }
  }
  WAITV(0);
  BARRIER();
  STAMP(4);
  if (wv == 7 && lane == 0) __hip_atomic_store((GAS u64*)(ws + WS_FB + (t2 * 16 + j) * 8), tag, __ATOMIC_RELAXED, __HIP_MEMORY_SCOPE_AGENT);
  const unsigned Tw = min(wv, 6u);
  const unsigned aI = (4 * q + qq) * 224 + 8 * p;
  bf16x8 wof[16];
  { const unsigned a0 = aI + 32 * Tw, a1 = a0 + (pad ? 0u : 3584u);
    UNR for (int kk = 0; kk < 16; kk++) { const unsigned b = kk < 7 ? L_WO1 + kk * 6272 : (kk - 7) * 6272; wof[kk] = trfrag(a0 + b, a1 + b); } }
  { u32x4 v[8]; const unsigned vo = (lane >> 5) * 8 * 4096 + (lane & 31) * 16, so = WS_KV + hd * 16 * 4096 + wv * 512;
    for (;;) { MEMFENCE(); unsigned m = 0;
      UNR for (int i = 0; i < 8; i++) v[i] = ldsc1(rS, vo, so + i * 4096);
      UNR for (int i = 0; i < 8; i++) m = vmax(v[i], m);
      if (!anybad(m) || ++spins > SPIN_CAP) break; }
    float a[8];
    UNR for (int e = 0; e < 4; e++) { a[2 * e] = blo(v[0][e]); a[2 * e + 1] = bhi(v[0][e]); }
    UNR for (int i = 1; i < 8; i++) UNR for (int e = 0; e < 4; e++) { a[2 * e] += blo(v[i][e]); a[2 * e + 1] += bhi(v[i][e]); }
    float b[4];
    UNR for (int e = 0; e < 4; e++) { auto sw = __builtin_amdgcn_permlane32_swap(f2u(a[e]), f2u(a[4 + e]), false, false); b[e] = u2f(sw[0]) + u2f(sw[1]); }
    *(LAS u32x2*)(L_KVF + (wv >> 1) * 1024 + (2 * (lane & 31) + (lane >> 5)) * 16 + (wv & 1) * 8) = u32x2{pk2(b[0], b[1]), pk2(b[2], b[3])};
  }
  BARRIER();
  STAMP(5);
  { const bf16x8 k0 = asb(ldsu4(L_KVF + lane * 16)), k1 = asb(ldsu4(L_KVF + 1024 + lane * 16)), k2 = asb(ldsu4(L_KVF + 2048 + lane * 16)), k3 = asb(ldsu4(L_KVF + 3072 + lane * 16));
    f32x4 o0 = mfma(k0, phq0, zero4), o1 = mfma(k1, phq0, zero4); o0 = mfma(k2, phq1, o0); o1 = mfma(k3, phq1, o1);
    const float rd = __builtin_amdgcn_rcpf(bcast_q3(o1[0]) + 1e-6f);
    stsc1(u32x4{pk2(o0[0] * rd, o0[1] * rd), pk2(o0[2] * rd, o0[3] * rd), pad ? 0u : pk2(o1[0] * rd, o1[1] * rd), pad ? 0u : pk2(o1[2] * rd, o1[3] * rd)}, rS, lane * 16, oslot);
  }
  STAMP(6);
  UNR for (int m = 0; m < 8; m++) { const unsigned n = wv + 8 * m; if (m < 7 || n < 63) glds(L_W1 + n * 1024, img_voff<3584>(n, lane), rW1, 224 * j); }
  const unsigned stg = L_STG + wv * 7168, w2s = (224 * wv) * 896 + 224 * c;
  UNR for (int m = 0; m < 7; m++) glds(stg + m * 1024, img_voff<896>(m, lane), rW2, w2s);
  u32x2 xr0, xr1, b1r, b2r;
  { const unsigned xo = (128 * t2 + 32 * s + r) * 896 + 224 * c + 32 * Tw + 8 * q;
    xr0 = __builtin_amdgcn_raw_buffer_load_b64(rX, xo, 0, 0); xr1 = __builtin_amdgcn_raw_buffer_load_b64(rX, xo, 16 * 896, 0);
    b1r = *(const GAS u32x2*)(b1 + 112 * j + 16 * Tw + 4 * q); b2r = *(const GAS u32x2*)(b2 + 112 * c + 16 * Tw + 4 * q); }
  if (wv == 7) {
    const unsigned fo = WS_FB + (t2 * 16 + r) * 8;
    for (;;) { MEMFENCE(); const u64 f = __hip_atomic_load((GAS u64*)(ws + fo), __ATOMIC_RELAXED, __HIP_MEMORY_SCOPE_AGENT);
      if (__builtin_amdgcn_ballot_w64(f != tag) == 0 || ++spins > SPIN_CAP) break; }
  }
  { u32x4 v[4]; const unsigned so = WS_O + ((t2 * 4 + s) * 16 + 2 * wv) * 2048;
    for (;;) { MEMFENCE(); unsigned m = 0;
      UNR for (int i = 0; i < 4; i++) v[i] = ldsc1(rS, lane * 16, so + i * 1024);
      UNR for (int i = 0; i < 4; i++) m = vmax(v[i], m);
      if (!anybad(m) || ++spins > SPIN_CAP) break; }
    UNR for (int i = 0; i < 4; i++) *(LAS u32x4*)(L_OST + (4 * wv + i) * 1024 + lane * 16) = v[i];
  }
  BARRIER();
  STAMP(7);
  if (w == 0 && wv == 7 && lane == 0) __hip_atomic_store((GAS u64*)ws, tag, __ATOMIC_RELAXED, __HIP_MEMORY_SCOPE_AGENT);
  f32x4 h0 = zero4, h1 = zero4;
  if (wv < 7) {
    UNR for (int kk = 0; kk < 16; kk++) { h0 = mfma(wof[kk], asb(ldsu4(L_OST + kk * 2048 + lane * 16)), h0); h1 = mfma(wof[kk], asb(ldsu4(L_OST + kk * 2048 + 1024 + lane * 16)), h1); }
    h0[0] += blo(xr0[0]); h0[1] += bhi(xr0[0]); h0[2] += blo(xr0[1]); h0[3] += bhi(xr0[1]);
    h1[0] += blo(xr1[0]); h1[1] += bhi(xr1[0]); h1[2] += blo(xr1[1]); h1[3] += bhi(xr1[1]);
    const unsigned TT = 7 * c + wv, so = gbase + G_H + (2 * s * 14 + (TT >> 1)) * 1024 + (TT & 1) * 8;
    stsc1_2(u32x2{pk2(h0[0], h0[1]), pk2(h0[2], h0[3])}, rS, lane * 16, so);
    stsc1_2(u32x2{pk2(h1[0], h1[1]), pk2(h1[2], h1[3])}, rS, lane * 16, so + 14 * 1024);
    const float s0 = allq_sum((h0[0] + h0[1]) + (h0[2] + h0[3])), s1 = allq_sum((h1[0] + h1[1]) + (h1[2] + h1[3]));
    const float q0 = allq_sum(h0[0] * h0[0] + h0[1] * h0[1] + h0[2] * h0[2] + h0[3] * h0[3]), q1 = allq_sum(h1[0] * h1[0] + h1[1] * h1[1] + h1[2] * h1[2] + h1[3] * h1[3]);
    if (q < 2) *(LAS f32x2*)(L_KVF + wv * 256 + lane * 8) = q ? f32x2{s1, q1} : f32x2{s0, q0};
  }
  STAMP(8);
  BARRIER();
  if (wv == 7 && lane < 32) { f32x2 a = *(LAS f32x2*)(L_KVF + lane * 8);
    UNR for (int i = 1; i < 7; i++) a += *(LAS f32x2*)(L_KVF + i * 256 + lane * 8);
    stsc1_2(u32x2{f2u(a[0]), f2u(a[1])}, rS, (lane >> 4) * 512 + (lane & 15) * 8, gbase + G_ST + 2 * s * 512 + c * 128); }
  UNR for (int m = 0; m < 5; m++) { const unsigned n = 63 + wv + 8 * m; if (m < 4 || n < 98) glds(L_W1 + n * 1024, img_voff<3584>(n, lane), rW1, 224 * j); }
  bf16x8 w2f[5][7];
#define W2CONV(B) do { UNR for (int T = 0; T < 7; T++) w2f[B][T] = trfrag(stg + aI + 32 * T, stg + aI + 32 * T + 3584); asm volatile("s_waitcnt lgkmcnt(0)" ::: "memory"); \
    UNR for (int T = 0; T < 7; T++) asm volatile("" : "+v"(w2f[B][T])); } while (0)
#define W2LOAD(B, DST) do { UNR for (int m = 0; m < 7; m++) glds((DST) + m * 1024, img_voff<896>(m, lane), rW2, w2s + (B) * 32 * 896); } while (0)
  W2CONV(0);
  W2LOAD(1, stg);
  WAITV(7);
  BARRIER();
  if (wv < 7) { f32x4 cs = zero4; const unsigned a = L_W1 + aI + 32 * wv;
    UNR for (int ks = 0; ks < 14; ks++) cs = mfma(trfrag(a + ks * 7168, a + ks * 7168 + 3584), ones, cs);
    if (r == 0) { *(LAS f32x4*)(L_CS + wv * 128 + q * 32) = cs; *(LAS f32x4*)(L_CS + wv * 128 + q * 32 + 16) = f32x4{blo(b1r[0]), bhi(b1r[0]), blo(b1r[1]), bhi(b1r[1])}; } }
  WAITV(0); W2CONV(1); W2LOAD(2, stg);
  BARRIER();
  STAMP(9);
  { u32x4 hf[14]; u32x2 st; const unsigned so = gbase + G_H + wv * 14 * 1024;
    for (;;) { MEMFENCE(); unsigned m = 0;
      UNR for (int i = 0; i < 14; i++) hf[i] = ldsc1(rS, lane * 16, so + i * 1024);
      UNR for (int i = 0; i < 14; i++) m = vmax(hf[i], m);
      if (!anybad(m) || ++spins > SPIN_CAP) break; }
    STAMP(10);
    W2CONV(2); W2LOAD(3, stg);
    f32x4 acc[7];
    UNR for (int T = 0; T < 7; T++) acc[T] = zero4;
    UNR for (int ks = 0; ks < 14; ks++) UNR for (int T = 0; T < 7; T++)
      acc[T] = mfma(trfrag(L_W1 + aI + ks * 7168 + 32 * T, L_W1 + aI + ks * 7168 + 32 * T + 3584), asb(hf[ks]), acc[T]);
    STAMP(11);
    for (;;) { MEMFENCE(); st = __builtin_amdgcn_raw_buffer_load_b64(rS, lane * 8, gbase + G_ST + wv * 512, 16);
      if (!anybad(max(st[0], st[1])) || ++spins > SPIN_CAP) break; }
    W2CONV(3); W2LOAD(4, stg);
    const float m = allq_sum(u2f(st[0])) * (1.f / 448.f), var = fmaxf(allq_sum(u2f(st[1])) * (1.f / 448.f) - m * m, 0.f);
    const float r1 = __builtin_amdgcn_rsqf(var + 1e-5f), R = r1 * __builtin_amdgcn_rsqf(var * r1 * r1 + 1e-5f), nm = -m * R;
    u32x2 g[7];
    UNR for (int T = 0; T < 7; T++) { const f32x4 cs = ldsf4(L_CS + T * 128 + q * 32), bb = ldsf4(L_CS + T * 128 + q * 32 + 16); float e[4];
      UNR for (int i = 0; i < 4; i++) { const float v = fmaf(acc[T][i], R, fmaf(nm, cs[i], bb[i]));
        const float C1 = -2.302208198f, C2 = C1 * 0.044715f;
        const float z = v * fmaf(v * v, C2, C1); e[i] = v * __builtin_amdgcn_rcpf(1.f + __builtin_amdgcn_exp2f(z)); }
      g[T] = u32x2{pk2(e[0], e[1]), pk2(e[2], e[3])}; }
    const unsigned ho = gbase + G_HH + (((wv >> 1) * 56 + ((7 * j) >> 1)) * 2 + (wv & 1)) * 1024;
    if (j & 1) { stsc1_2(g[0], rS, lane * 16 + 8, ho);
      UNR for (int m2 = 0; m2 < 3; m2++) stsc1(u32x4{g[1 + 2 * m2][0], g[1 + 2 * m2][1], g[2 + 2 * m2][0], g[2 + 2 * m2][1]}, rS, lane * 16, ho + (m2 + 1) * 2048);
    } else {
      UNR for (int m2 = 0; m2 < 3; m2++) stsc1(u32x4{g[2 * m2][0], g[2 * m2][1], g[2 * m2 + 1][0], g[2 * m2 + 1][1]}, rS, lane * 16, ho + m2 * 2048);
      stsc1_2(g[6], rS, lane * 16, ho + 3 * 2048); }
  }
  STAMP(12);
  WAITV(4); W2CONV(4); W2LOAD(5, stg);
  BARRIER();
  const unsigned stg2 = wv * 7168;
  W2LOAD(6, stg2);
  { u32x4 hf[14]; const unsigned so = gbase + G_HH + (s * 56 + 7 * wv) * 2048;
    for (;;) { MEMFENCE(); unsigned m = 0;
      UNR for (int i = 0; i < 14; i++) hf[i] = ldsc1(rS, lane * 16, so + i * 1024);
      UNR for (int i = 0; i < 14; i++) m = vmax(hf[i], m);
      if (!anybad(m) || ++spins > SPIN_CAP) break; }
    STAMP(13);
    f32x4 acc[7][2];
    UNR for (int T = 0; T < 7; T++) { acc[T][0] = zero4; acc[T][1] = zero4; }
    UNR for (int kb = 0; kb < 7; kb++) UNR for (int T = 0; T < 7; T++) {
      const bf16x8 a = kb < 5 ? w2f[kb < 5 ? kb : 0][T] : trfrag((kb == 5 ? stg : stg2) + aI + 32 * T, (kb == 5 ? stg : stg2) + aI + 32 * T + 3584);
      acc[T][0] = mfma(a, asb(hf[2 * kb]), acc[T][0]); acc[T][1] = mfma(a, asb(hf[2 * kb + 1]), acc[T][1]); }
    MEMFENCE();
    UNR for (int u = 0; u < 14; u++) *(LAS f32x4*)((u < 7 ? stg + u * 1024 : stg2 + (u - 7) * 1024) + lane * 16) = acc[u >> 1][u & 1];
  }
  STAMP(14);
  BARRIER();
  if (wv < 7) {
    const unsigned u0 = 2 * wv, u1 = u0 + 1;
    const unsigned a0 = (u0 < 7 ? L_STG + u0 * 1024 : (u0 - 7) * 1024) + lane * 16, a1 = (u1 < 7 ? L_STG + u1 * 1024 : (u1 - 7) * 1024) + lane * 16;
    const f32x4 bb = {blo(b2r[0]), bhi(b2r[0]), blo(b2r[1]), bhi(b2r[1])};
    h0 += bb; h1 += bb;
    UNR for (int i = 0; i < 8; i++) { h0 += ldsf4(a0 + i * 7168); h1 += ldsf4(a1 + i * 7168); }
    GAS u16* o = (GAS u16*)out + (128 * t2 + 32 * s + r) * 448 + 112 * c + 16 * wv + 4 * q;
    *(GAS u32x2*)o = u32x2{pk2(h0[0], h0[1]), pk2(h0[2], h0[3])};
    *(GAS u32x2*)(o + 16 * 448) = u32x2{pk2(h1[0], h1[1]), pk2(h1[2], h1[3])};
  }
  STAMP(15); CSTAMP(31);
}

extern "C" void kernel_launch(void* const* d_in, const int* in_sizes, int n_in, void* d_out, int out_size, void* d_ws, size_t ws_size, hipStream_t stream) {
  (void)hipFuncSetAttribute((const void*)fused_kernel, hipFuncAttributeMaxDynamicSharedMemorySize, LDS_BYTES);
  fused_kernel<<<256, 512, LDS_BYTES, stream>>>((const u16*)d_in[0], (const u16*)d_in[1], (const u16*)d_in[2], (const u16*)d_in[3], (const u16*)d_in[5], (const u16*)d_in[6],
                                                (const u16*)d_in[7], (const u16*)d_in[8], (u16*)d_out, (char*)d_ws);
}
```

```cpp
#define THR 44.0f
#include <hip/hip_runtime.h>
#include <hip/hip_bf16.h>

typedef unsigned short u16;
typedef unsigned long long u64;
typedef __bf16 bf16x2 __attribute__((ext_vector_type(2)));
typedef __bf16 bf16x4 __attribute__((ext_vector_type(4)));
typedef __bf16 bf16x8 __attribute__((ext_vector_type(8)));
typedef float f32x2 __attribute__((ext_vector_type(2)));
typedef float f32x4 __attribute__((ext_vector_type(4)));
typedef unsigned u32x2 __attribute__((ext_vector_type(2)));
typedef unsigned u32x4 __attribute__((ext_vector_type(4)));
typedef __amdgpu_buffer_rsrc_t rsrc_t;
#define LAS __attribute__((address_space(3)))
#define GAS __attribute__((address_space(1)))
#define CAS __attribute__((address_space(4)))
#define DI __device__ __forceinline__
#define UNR _Pragma("unroll")

constexpr unsigned WS_FA1 = 4096, WS_FA2 = 8192, WS_FB = 12288;
constexpr unsigned WS_KV = 65536;
constexpr unsigned WS_O = WS_KV + (1u << 20);
constexpr unsigned WS_G = WS_O + (2u << 20);
constexpr unsigned G_H = 0, G_ST = 114688, G_HH = 118784, G_SZ = 589824;
constexpr unsigned WS_END = WS_G + 16 * G_SZ;
constexpr unsigned WS_DBG = 16u << 20;
constexpr unsigned L_WK = 0, L_WV = 28672, L_WQ = 57344, L_WO1 = 86016, L_PV = 130048, L_KVF = 158720, L_CS = 162816;
constexpr unsigned L_W1 = 0, L_OST = 65536, L_STG = 100352, LDS_BYTES = 163840;
constexpr unsigned SENT = 0xFFFFFFFFu;
constexpr int SPIN_CAP = 1 << 19;

DI unsigned rfl(unsigned v) { return __builtin_amdgcn_readfirstlane(v); }
DI rsrc_t mkrsrc(const void* p, unsigned bytes) { return __builtin_amdgcn_make_buffer_rsrc((void*)p, (short)0, (int)bytes, 0x00020000); }
DI bf16x4 trd(unsigned a) { return __builtin_amdgcn_ds_read_tr16_b64_v4bf16((LAS bf16x4*)a); }
DI bf16x8 cat(bf16x4 a, bf16x4 b) { return __builtin_shufflevector(a, b, 0, 1, 2, 3, 4, 5, 6, 7); }
DI bf16x8 trfrag(unsigned a0, unsigned a1) { return cat(trd(a0), trd(a1)); }
DI f32x4 mfma(bf16x8 a, bf16x8 b, f32x4 c) { return __builtin_amdgcn_mfma_f32_16x16x32_bf16(a, b, c, 0, 0, 0); }
DI unsigned pk2(float a, float b) { f32x2 f = {a, b}; return __builtin_bit_cast(unsigned, __builtin_convertvector(f, bf16x2)); }
DI bf16x8 asb(u32x4 v) { return __builtin_bit_cast(bf16x8, v); }
DI float blo(unsigned u) { return __builtin_bit_cast(float, u << 16); }
DI float bhi(unsigned u) { return __builtin_bit_cast(float, u & 0xffff0000u); }
DI unsigned f2u(float f) { return __builtin_bit_cast(unsigned, f); }
DI float u2f(unsigned u) { return __builtin_bit_cast(float, u); }
DI float allq_sum(float v) {
  auto a = __builtin_amdgcn_permlane32_swap(f2u(v), f2u(v), false, false); float t = u2f(a[0]) + u2f(a[1]);
  auto b = __builtin_amdgcn_permlane16_swap(f2u(t), f2u(t), false, false); return u2f(b[0]) + u2f(b[1]);
}
DI float allq_max(float v) {
  auto a = __builtin_amdgcn_permlane32_swap(f2u(v), f2u(v), false, false); float t = fmaxf(u2f(a[0]), u2f(a[1]));
  auto b = __builtin_amdgcn_permlane16_swap(f2u(t), f2u(t), false, false); return fmaxf(u2f(b[0]), u2f(b[1]));
}
DI float bcast_q3(float v) {
  auto a = __builtin_amdgcn_permlane32_swap(f2u(v), f2u(v), false, false);
  auto b = __builtin_amdgcn_permlane16_swap(a[1], a[1], false, false); return u2f(b[1]);
}
DI unsigned umax3(unsigned a, unsigned b, unsigned c) { return max(max(a, b), c); }
DI unsigned vmax(u32x4 v, unsigned m) { return umax3(umax3(v.x, v.y, v.z), v.w, m); }
DI bool anybad(unsigned m) { return __builtin_amdgcn_ballot_w64(m == SENT) != 0; }
#define WAITV(n) asm volatile("s_waitcnt vmcnt(%0)" :: "i"(n) : "memory")
#define BARRIER() asm volatile("s_waitcnt lgkmcnt(0)\n\ts_barrier" ::: "memory")
#define MEMFENCE() asm volatile("" ::: "memory")
DI void glds(unsigned ldsa, unsigned voff, rsrc_t rs, unsigned soff) {
  asm volatile("s_mov_b32 m0, %0\n\ts_nop 0\n\tbuffer_load_dwordx4 %1, %2, %3 offen lds" :: "s"(ldsa), "v"(voff), "s"(rs), "s"(soff) : "memory");
}
DI void glds_nt(unsigned ldsa, unsigned voff, rsrc_t rs, unsigned soff) {
  asm volatile("s_mov_b32 m0, %0\n\ts_nop 0\n\tbuffer_load_dwordx4 %1, %2, %3 offen nt lds" :: "s"(ldsa), "v"(voff), "s"(rs), "s"(soff) : "memory");
}
template <unsigned RS> DI unsigned img_voff(unsigned i, unsigned lane) { unsigned n = i * 64 + lane, row = (n * 9363u) >> 17; return row * RS + (n - row * 14u) * 16u; }
DI u32x4 ldsc1(rsrc_t rs, unsigned voff, unsigned soff) { return __builtin_amdgcn_raw_buffer_load_b128(rs, voff, soff, 16); }
DI void stsc1(u32x4 v, rsrc_t rs, unsigned voff, unsigned soff) { __builtin_amdgcn_raw_buffer_store_b128(v, rs, voff, soff, 16); asm volatile("s_nop 1" :: "v"(v)); }
DI void stsc1_2(u32x2 v, rsrc_t rs, unsigned voff, unsigned soff) { __builtin_amdgcn_raw_buffer_store_b64(v, rs, voff, soff, 16); }
DI f32x4 ldsf4(unsigned a) { return *(LAS f32x4*)a; }
DI u32x4 ldsu4(unsigned a) { return *(LAS u32x4*)a; }

#define NSTAMP 16
#define STAMP(k) do {} while (0)
#define CSTAMP(k) do {} while (0)

DI void featmap(const f32x4 a0, const f32x4 a1, const bf16x8 ph0, const bf16x8 ph1, const bool pad, float (&ep)[8], float (&en)[8]) {
  u32x4 kb = {pk2(a0[0], a0[1]), pk2(a0[2], a0[3]), pad ? 0u : pk2(a1[0], a1[1]), pad ? 0u : pk2(a1[2], a1[3])};
  const f32x4 z = {0.f, 0.f, 0.f, 0.f};
  f32x4 p0 = mfma(ph0, asb(kb), z), p1 = mfma(ph1, asb(kb), z);
  float p[8];
  UNR for (int i = 0; i < 4; i++) { p[i] = p0[i]; p[4 + i] = pad ? p0[i] : p1[i]; }
  float mx = fmaxf(fmaxf(fmaxf(p[0], p[1]), fmaxf(p[2], p[3])), fmaxf(fmaxf(p[4], p[5]), fmaxf(p[6], p[7])));
  float mn = fminf(fminf(fminf(p[0], p[1]), fminf(p[2], p[3])), fminf(fminf(p[4], p[5]), fminf(p[6], p[7])));
  mx = allq_max(mx); mn = -allq_max(-mn);
  const float L2E = 1.4426950408889634f;
  const float cx = -mx * L2E, cn = mn * L2E;
  float sp = 0.f, sn = 0.f;
  UNR for (int i = 0; i < 8; i++) {
    float e1 = __builtin_amdgcn_exp2f(fmaf(p[i], L2E, cx)), e2 = __builtin_amdgcn_exp2f(fmaf(p[i], -L2E, cn));
    if (i >= 4) { e1 = pad ? 0.f : e1; e2 = pad ? 0.f : e2; }
    ep[i] = e1; en[i] = e2; sp += e1; sn += e2;
  }
  sp = __builtin_amdgcn_rcpf(allq_sum(sp)); sn = __builtin_amdgcn_rcpf(allq_sum(sn));
  UNR for (int i = 0; i < 8; i++) { ep[i] *= sp; en[i] *= sn; }
}

__global__ void __launch_bounds__(512, 2) fused_kernel(const u16* __restrict__ x, const u16* __restrict__ wqkv, const u16* __restrict__ wphi, const u16* __restrict__ wo,
                                                       const u16* __restrict__ w1, const u16* __restrict__ b1, const u16* __restrict__ w2, const u16* __restrict__ b2,
                                                       u16* __restrict__ out, char* __restrict__ ws) {
  const unsigned tid = threadIdx.x, lane = tid & 63, wv = rfl(tid >> 6);
  const unsigned q = lane >> 4, r = lane & 15, qq = (lane >> 2) & 3, p = lane & 3;
  const unsigned w = blockIdx.x, x8 = w & 7, ii = w >> 3;
  const unsigned t1 = 4 * (x8 >> 1) + (ii >> 3), hd = 8 * (x8 & 1) + (ii & 7);
  const unsigned t2 = 2 * x8 + (ii >> 4), j = ii & 15, s = j >> 2, c = j & 3;
  const bool pad = (q == 3);
  u64 tag = *(const CAS u64*)ws + 1ull;
  asm volatile("" : "+s"(tag));
  const rsrc_t rX = mkrsrc(x, 2048 * 896), rW = mkrsrc(wqkv, 448 * 2688), rP = mkrsrc(wphi, 16 * 1568), rWO = mkrsrc(wo, 448 * 896);
  const rsrc_t rW1 = mkrsrc(w1, 448 * 3584), rW2 = mkrsrc(w2, 1792 * 896), rS = mkrsrc(ws, WS_END);
  const unsigned gbase = WS_G + t2 * G_SZ;
  const u32x4 sent4 = {SENT, SENT, SENT, SENT};
  const f32x4 zero4 = {0.f, 0.f, 0.f, 0.f};
  const u32x4 ones_u = {0x3f803f80u, 0x3f803f80u, 0x3f803f80u, 0x3f803f80u};
  const bf16x8 ones = asb(ones_u);
  int spins = 0;
  const u64 tstart = __builtin_amdgcn_s_memrealtime();

  const unsigned kvslot = WS_KV + (hd * 16 + t1) * 4096 + wv * 512, oslot = WS_O + (((t1 * 4 + (wv >> 1)) * 16 + hd) * 2 + (wv & 1)) * 1024;
  stsc1_2(u32x2{SENT, SENT}, rS, lane * 8, kvslot);
  stsc1(sent4, rS, lane * 16, oslot);
  if (wv < 2) glds(L_KVF + wv * 1024, lane * 16, rP, hd * 1568 + wv * 1024);
  const unsigned wvoff = (lane >> 2) * 2688 + (((lane & 3) ^ ((lane >> 5) << 1)) << 4), wwin = (56 * hd) & ~15u;
  UNR for (int m = 0; m < 7; m++) { const unsigned n = wv + 8 * m, sec = n >= 28; glds(n * 1024, wvoff, rW, (n - 28 * sec) * (16 * 2688) + (sec + 1) * 896 + wwin); }
  u32x4 xf[14];
  { const unsigned xv = (128 * t1 + 16 * wv + r) * 896 + q * 16;
    UNR for (int ks = 0; ks < 14; ks++) asm volatile("buffer_load_dwordx4 %0, %1, %2, 0 offen offset:%3" : "=v"(xf[ks]) : "v"(xv), "s"(rX), "i"(ks * 64)); }
  UNR for (int m = 0; m < 4; m++) { const unsigned n = min(wv + 8 * m, 27u); glds(L_WQ + n * 1024, wvoff, rW, n * (16 * 2688) + wwin); }
  UNR for (int m = 0; m < 5; m++) { const unsigned n = wv + 8 * m; stsc1(sent4, rS, lane * 16, n < 36 ? gbase + (j * 36 + n) * 1024 : 0xF0000000u); }
  UNR for (int m = 0; m < 6; m++) { const unsigned n = wv + 8 * m; if (m < 5 || n < 43) glds(L_WO1 + n * 1024, img_voff<896>(n, lane), rWO, 224 * c); }
  constexpr int NY = 14;
  WAITV(14 + NY);
  BARRIER();
  STAMP(1);
  if (wv == 7 && lane < 2) __hip_atomic_store((GAS u64*)(ws + (lane ? WS_FA2 + (t1 * 16 + hd) * 8 : WS_FA1 + (hd * 16 + t1) * 8)), tag, __ATOMIC_RELAXED, __HIP_MEMORY_SCOPE_AGENT);
  bf16x8 ph0, ph1;
  { const unsigned r0 = (4 * q + qq) * 56, r1 = (pad ? 4 * q + qq : 16 + 4 * q + qq) * 56;
    ph0 = trfrag(L_KVF + r0 + 8 * p, L_KVF + r1 + 8 * p); ph1 = trfrag(L_KVF + r0 + 32 + 8 * p, L_KVF + r1 + 32 + 8 * p); }
  const unsigned sh = 8 * (hd & 1), wrow = (8 * q + qq) * 64;
  const unsigned aW0 = wrow + ((8 * p + sh) ^ ((q & 1) << 5)), aW1 = wrow + (min(32 + 8 * p + sh, 56u) ^ ((q & 1) << 5));
  if (wv < 4) {
    const unsigned a = ((wv & 1) ? aW1 : aW0) + (wv >> 1) * 28672; f32x4 cs = zero4;
    UNR for (int ks = 0; ks < 14; ks++) cs = mfma(trfrag(a + ks * 2048, a + ks * 2048 + 256), ones, cs);
    if (r == 0) *(LAS f32x4*)(L_CS + wv * 64 + q * 16) = cs;
  }
  f32x4 aK0 = zero4, aK1 = zero4, aV0 = zero4, aV1 = zero4, aS = zero4, aQ = zero4;
  UNR for (int ks = 0; ks < 14; ks++) {
    asm volatile("s_waitcnt vmcnt(%1)" : "+v"(xf[ks]) : "i"(13 - ks + NY));
    const bf16x8 xb = asb(xf[ks]);
    aK0 = mfma(trfrag(aW0 + L_WK + ks * 2048, aW0 + L_WK + ks * 2048 + 256), xb, aK0);
    aK1 = mfma(trfrag(aW1 + L_WK + ks * 2048, aW1 + L_WK + ks * 2048 + 256), xb, aK1);
    aV0 = mfma(trfrag(aW0 + L_WV + ks * 2048, aW0 + L_WV + ks * 2048 + 256), xb, aV0);
    aV1 = mfma(trfrag(aW1 + L_WV + ks * 2048, aW1 + L_WV + ks * 2048 + 256), xb, aV1);
    aS = mfma(ones, xb, aS); aQ = mfma(xb, xb, aQ);
  }
  float mean, rstd;
  { const unsigned ri = r & 3; float d = ri == 0 ? aQ[0] : ri == 1 ? aQ[1] : ri == 2 ? aQ[2] : aQ[3]; d = (q == (r >> 2)) ? d : 0.f;
    const float sq = allq_sum(d); mean = aS[0] * (1.f / 448.f); const float var = fmaxf(sq * (1.f / 448.f) - mean * mean, 0.f); rstd = __builtin_amdgcn_rsqf(var + 1e-5f); }
  WAITV(10);
  BARRIER();
  STAMP(2);
  { const float nm = -mean * rstd;
    const f32x4 c0 = ldsf4(L_CS + q * 16), c1 = ldsf4(L_CS + 64 + q * 16), c2 = ldsf4(L_CS + 128 + q * 16), c3 = ldsf4(L_CS + 192 + q * 16);
    UNR for (int i = 0; i < 4; i++) { aK0[i] = fmaf(aK0[i], rstd, nm * c0[i]); aK1[i] = fmaf(aK1[i], rstd, nm * c1[i]); aV0[i] = fmaf(aV0[i], rstd, nm * c2[i]); aV1[i] = fmaf(aV1[i], rstd, nm * c3[i]); }
    float ep[8], en[8];
    featmap(aK0, aK1, ph0, ph1, pad, ep, en);
    const unsigned a = L_PV + (16 * wv + r) * 224 + ((q ^ (r >> 2)) << 3);
    *(LAS u32x2*)(a) = u32x2{pk2(ep[0], ep[1]), pk2(ep[2], ep[3])};
    *(LAS u32x2*)(a + 32) = u32x2{pk2(ep[4], ep[5]), pk2(ep[6], ep[7])};
    *(LAS u32x2*)(a + 64) = u32x2{pk2(en[0], en[1]), pk2(en[2], en[3])};
    *(LAS u32x2*)(a + 96) = u32x2{pk2(en[4], en[5]), pk2(en[6], en[7])};
    *(LAS u32x2*)(a + 128) = u32x2{pk2(aV0[0], aV0[1]), pk2(aV0[2], aV0[3])};
    *(LAS u32x2*)(a + 160) = u32x2{pad ? 0x00003f80u : pk2(aV1[0], aV1[1]), pad ? 0u : pk2(aV1[2], aV1[3])};
  }
  BARRIER();
  { const unsigned ab = L_PV + (4 * q + qq) * 224 + ((p ^ q) << 3), aA = ab + (2 * (wv >> 2) + (wv & 1)) * 32, aB = ab + (4 + ((wv >> 1) & 1)) * 32;
    f32x4 kv = zero4;
    UNR for (int kk = 0; kk < 4; kk++) kv = mfma(trfrag(aA + kk * 7168, aA + kk * 7168 + 3584), trfrag(aB + kk * 7168, aB + kk * 7168 + 3584), kv);
    stsc1_2(u32x2{pk2(kv[0], kv[1]), pk2(kv[2], kv[3])}, rS, lane * 8, kvslot);
  }
  STAMP(3);
  UNR for (int m = 0; m < 7; m++) { const unsigned n = wv + 8 * m; glds(n * 1024, img_voff<896>(n, lane), rWO, 196 * 896 + 224 * c); }
  bf16x8 phq0, phq1;
  { f32x4 aQ0 = zero4, aQ1 = zero4;
    const unsigned a0 = aW0 + L_WQ, a1 = aW1 + L_WQ;
    if (wv >= 6) { const unsigned a = (wv & 1) ? a1 : a0; f32x4 cs = zero4;
      UNR for (int ks = 0; ks < 14; ks++) cs = mfma(trfrag(a + ks * 2048, a + ks * 2048 + 256), ones, cs);
      if (r == 0) *(LAS f32x4*)(L_CS + 256 + (wv & 1) * 64 + q * 16) = cs; }
    UNR for (int ks = 0; ks < 14; ks++) { const bf16x8 xb = asb(xf[ks]);
      aQ0 = mfma(trfrag(a0 + ks * 2048, a0 + ks * 2048 + 256), xb, aQ0); aQ1 = mfma(trfrag(a1 + ks * 2048, a1 + ks * 2048 + 256), xb, aQ1); }
    BARRIER();
    const float nm = -mean * rstd; const f32x4 c0 = ldsf4(L_CS + 256 + q * 16), c1 = ldsf4(L_CS + 320 + q * 16);
    UNR for (int i = 0; i < 4; i++) { aQ0[i] = fmaf(aQ0[i], rstd, nm * c0[i]); aQ1[i] = fmaf(aQ1[i], rstd, nm * c1[i]); }
    float ep[8], en[8];
    featmap(aQ0, aQ1, ph0, ph1, pad, ep, en);
    phq0 = asb(u32x4{pk2(ep[0], ep[1]), pk2(ep[2], ep[3]), pk2(ep[4], ep[5]), pk2(ep[6], ep[7])});
    phq1 = asb(u32x4{pk2(en[0], en[1]), pk2(en[2], en[3]), pk2(en[4], en[5]), pk2(en[6], en[7])});
  }
  if (wv == 7) {
    const unsigned fo = lane < 16 ? WS_FA1 + (hd * 16 + lane) * 8 : WS_FA2 + (t2 * 16 + (lane & 15)) * 8;
    for (;;) { MEMFENCE(); const u64 f = __hip_atomic_load((GAS u64*)(ws + fo), __ATOMIC_RELAXED, __HIP_MEMORY_SCOPE_AGENT);
      if (__builtin_amdgcn_ballot_w64(f != tag) == 0 || ++spins > SPIN_CAP) break; }
  }
  WAITV(0);
  BARRIER();
  STAMP(4);
  if (wv == 7 && lane == 0) __hip_atomic_store((GAS u64*)(ws + WS_FB + (t2 * 16 + j) * 8), tag, __ATOMIC_RELAXED, __HIP_MEMORY_SCOPE_AGENT);
  const unsigned Tw = min(wv, 6u);
  const unsigned aI = (4 * q + qq) * 224 + 8 * p;
  bf16x8 wof[16];
  { const unsigned a0 = aI + 32 * Tw, a1 = a0 + (pad ? 0u : 3584u);
    UNR for (int kk = 0; kk < 16; kk++) { const unsigned b = kk < 7 ? L_WO1 + kk * 6272 : (kk - 7) * 6272; wof[kk] = trfrag(a0 + b, a1 + b); } }
  { u32x4 v[8]; const unsigned vo = (lane >> 5) * 8 * 4096 + (lane & 31) * 16, so = WS_KV + hd * 16 * 4096 + wv * 512;
    for (;;) { MEMFENCE(); unsigned m = 0;
      UNR for (int i = 0; i < 8; i++) v[i] = ldsc1(rS, vo, so + i * 4096);
      UNR for (int i = 0; i < 8; i++) m = vmax(v[i], m);
      if (!anybad(m) || ++spins > SPIN_CAP) break; }
    float a[8];
    UNR for (int e = 0; e < 4; e++) { a[2 * e] = blo(v[0][e]); a[2 * e + 1] = bhi(v[0][e]); }
    UNR for (int i = 1; i < 8; i++) UNR for (int e = 0; e < 4; e++) { a[2 * e] += blo(v[i][e]); a[2 * e + 1] += bhi(v[i][e]); }
    float b[4];
    UNR for (int e = 0; e < 4; e++) { auto sw = __builtin_amdgcn_permlane32_swap(f2u(a[e]), f2u(a[4 + e]), false, false); b[e] = u2f(sw[0]) + u2f(sw[1]); }
    *(LAS u32x2*)(L_KVF + (wv >> 1) * 1024 + (2 * (lane & 31) + (lane >> 5)) * 16 + (wv & 1) * 8) = u32x2{pk2(b[0], b[1]), pk2(b[2], b[3])};
  }
  BARRIER();
  STAMP(5);
  { const bf16x8 k0 = asb(ldsu4(L_KVF + lane * 16)), k1 = asb(ldsu4(L_KVF + 1024 + lane * 16)), k2 = asb(ldsu4(L_KVF + 2048 + lane * 16)), k3 = asb(ldsu4(L_KVF + 3072 + lane * 16));
    f32x4 o0 = mfma(k0, phq0, zero4), o1 = mfma(k1, phq0, zero4); o0 = mfma(k2, phq1, o0); o1 = mfma(k3, phq1, o1);
    const float rd = __builtin_amdgcn_rcpf(bcast_q3(o1[0]) + 1e-6f);
    stsc1(u32x4{pk2(o0[0] * rd, o0[1] * rd), pk2(o0[2] * rd, o0[3] * rd), pad ? 0u : pk2(o1[0] * rd, o1[1] * rd), pad ? 0u : pk2(o1[2] * rd, o1[3] * rd)}, rS, lane * 16, oslot);
  }
  STAMP(6);
  UNR for (int m = 0; m < 8; m++) { const unsigned n = wv + 8 * m; if (m < 7 || n < 63) glds(L_W1 + n * 1024, img_voff<3584>(n, lane), rW1, 224 * j); }
  const unsigned stg = L_STG + wv * 7168, w2s = (224 * wv) * 896 + 224 * c;
  UNR for (int m = 0; m < 7; m++) glds(stg + m * 1024, img_voff<896>(m, lane), rW2, w2s);
  u32x2 xr0, xr1, b1r, b2r;
  { const unsigned xo = (128 * t2 + 32 * s + r) * 896 + 224 * c + 32 * Tw + 8 * q;
    xr0 = __builtin_amdgcn_raw_buffer_load_b64(rX, xo, 0, 0); xr1 = __builtin_amdgcn_raw_buffer_load_b64(rX, xo, 16 * 896, 0);
    b1r = *(const GAS u32x2*)(b1 + 112 * j + 16 * Tw + 4 * q); b2r = *(const GAS u32x2*)(b2 + 112 * c + 16 * Tw + 4 * q); }
  if (wv == 7) {
    const unsigned fo = WS_FB + (t2 * 16 + r) * 8;
    for (;;) { MEMFENCE(); const u64 f = __hip_atomic_load((GAS u64*)(ws + fo), __ATOMIC_RELAXED, __HIP_MEMORY_SCOPE_AGENT);
      if (__builtin_amdgcn_ballot_w64(f != tag) == 0 || ++spins > SPIN_CAP) break; }
  }
  { u32x4 v[4]; const unsigned so = WS_O + ((t2 * 4 + s) * 16 + 2 * wv) * 2048;
    for (;;) { MEMFENCE(); unsigned m = 0;
      UNR for (int i = 0; i < 4; i++) v[i] = ldsc1(rS, lane * 16, so + i * 1024);
      UNR for (int i = 0; i < 4; i++) m = vmax(v[i], m);
      if (!anybad(m) || ++spins > SPIN_CAP) break; }
    UNR for (int i = 0; i < 4; i++) *(LAS u32x4*)(L_OST + (4 * wv + i) * 1024 + lane * 16) = v[i];
  }
  BARRIER();
  STAMP(7);
  if (w == 0 && wv == 7 && lane == 0) __hip_atomic_store((GAS u64*)ws, tag, __ATOMIC_RELAXED, __HIP_MEMORY_SCOPE_AGENT);
  f32x4 h0 = zero4, h1 = zero4;
  if (wv < 7) {
    UNR for (int kk = 0; kk < 16; kk++) { h0 = mfma(wof[kk], asb(ldsu4(L_OST + kk * 2048 + lane * 16)), h0); h1 = mfma(wof[kk], asb(ldsu4(L_OST + kk * 2048 + 1024 + lane * 16)), h1); }
    h0[0] += blo(xr0[0]); h0[1] += bhi(xr0[0]); h0[2] += blo(xr0[1]); h0[3] += bhi(xr0[1]);
    h1[0] += blo(xr1[0]); h1[1] += bhi(xr1[0]); h1[2] += blo(xr1[1]); h1[3] += bhi(xr1[1]);
    const unsigned TT = 7 * c + wv, so = gbase + G_H + (2 * s * 14 + (TT >> 1)) * 1024 + (TT & 1) * 8;
    stsc1_2(u32x2{pk2(h0[0], h0[1]), pk2(h0[2], h0[3])}, rS, lane * 16, so);
    stsc1_2(u32x2{pk2(h1[0], h1[1]), pk2(h1[2], h1[3])}, rS, lane * 16, so + 14 * 1024);
    const float s0 = allq_sum((h0[0] + h0[1]) + (h0[2] + h0[3])), s1 = allq_sum((h1[0] + h1[1]) + (h1[2] + h1[3]));
    const float q0 = allq_sum(h0[0] * h0[0] + h0[1] * h0[1] + h0[2] * h0[2] + h0[3] * h0[3]), q1 = allq_sum(h1[0] * h1[0] + h1[1] * h1[1] + h1[2] * h1[2] + h1[3] * h1[3]);
    if (q < 2) *(LAS f32x2*)(L_KVF + wv * 256 + lane * 8) = q ? f32x2{s1, q1} : f32x2{s0, q0};
  }
  STAMP(8);
  BARRIER();
  if (wv == 7 && lane < 32) { f32x2 a = *(LAS f32x2*)(L_KVF + lane * 8);
    UNR for (int i = 1; i < 7; i++) a += *(LAS f32x2*)(L_KVF + i * 256 + lane * 8);
    stsc1_2(u32x2{f2u(a[0]), f2u(a[1])}, rS, (lane >> 4) * 512 + (lane & 15) * 8, gbase + G_ST + 2 * s * 512 + c * 128); }
  UNR for (int m = 0; m < 5; m++) { const unsigned n = 63 + wv + 8 * m; if (m < 4 || n < 98) glds(L_W1 + n * 1024, img_voff<3584>(n, lane), rW1, 224 * j); }
  bf16x8 w2f[5][7];
#define W2CONV(B) do { UNR for (int T = 0; T < 7; T++) w2f[B][T] = trfrag(stg + aI + 32 * T, stg + aI + 32 * T + 3584); asm volatile("s_waitcnt lgkmcnt(0)" ::: "memory"); \
    UNR for (int T = 0; T < 7; T++) asm volatile("" : "+v"(w2f[B][T])); } while (0)
#define W2LOAD(B, DST) do { UNR for (int m = 0; m < 7; m++) glds((DST) + m * 1024, img_voff<896>(m, lane), rW2, w2s + (B) * 32 * 896); } while (0)
  W2CONV(0);
  W2LOAD(1, stg);
  WAITV(7);
  BARRIER();
  if (wv < 7) { f32x4 cs = zero4; const unsigned a = L_W1 + aI + 32 * wv;
    UNR for (int ks = 0; ks < 14; ks++) cs = mfma(trfrag(a + ks * 7168, a + ks * 7168 + 3584), ones, cs);
    if (r == 0) { *(LAS f32x4*)(L_CS + wv * 128 + q * 32) = cs; *(LAS f32x4*)(L_CS + wv * 128 + q * 32 + 16) = f32x4{blo(b1r[0]), bhi(b1r[0]), blo(b1r[1]), bhi(b1r[1])}; } }
  WAITV(0); W2CONV(1); W2LOAD(2, stg);
  BARRIER();
  STAMP(9);
  { u32x4 hf[14]; u32x2 st; const unsigned so = gbase + G_H + wv * 14 * 1024;
    for (;;) { MEMFENCE(); unsigned m = 0;
      UNR for (int i = 0; i < 14; i++) hf[i] = ldsc1(rS, lane * 16, so + i * 1024);
      UNR for (int i = 0; i < 14; i++) m = vmax(hf[i], m);
      if (!anybad(m) || ++spins > SPIN_CAP) break; }
    STAMP(10);
    W2CONV(2); W2LOAD(3, stg);
    f32x4 acc[7];
    UNR for (int T = 0; T < 7; T++) acc[T] = zero4;
    UNR for (int ks = 0; ks < 14; ks++) UNR for (int T = 0; T < 7; T++)
      acc[T] = mfma(trfrag(L_W1 + aI + ks * 7168 + 32 * T, L_W1 + aI + ks * 7168 + 32 * T + 3584), asb(hf[ks]), acc[T]);
    STAMP(11);
    for (;;) { MEMFENCE(); st = __builtin_amdgcn_raw_buffer_load_b64(rS, lane * 8, gbase + G_ST + wv * 512, 16);
      if (!anybad(max(st[0], st[1])) || ++spins > SPIN_CAP) break; }
    W2CONV(3); W2LOAD(4, stg);
    const float m = allq_sum(u2f(st[0])) * (1.f / 448.f), var = fmaxf(allq_sum(u2f(st[1])) * (1.f / 448.f) - m * m, 0.f);
    const float r1 = __builtin_amdgcn_rsqf(var + 1e-5f), R = r1 * __builtin_amdgcn_rsqf(var * r1 * r1 + 1e-5f), nm = -m * R;
    u32x2 g[7];
    UNR for (int T = 0; T < 7; T++) { const f32x4 cs = ldsf4(L_CS + T * 128 + q * 32), bb = ldsf4(L_CS + T * 128 + q * 32 + 16); float e[4];
      UNR for (int i = 0; i < 4; i++) { const float v = fmaf(acc[T][i], R, fmaf(nm, cs[i], bb[i]));
        const float C1 = -2.302208198f, C2 = C1 * 0.044715f;
        const float z = v * fmaf(v * v, C2, C1); e[i] = v * __builtin_amdgcn_rcpf(1.f + __builtin_amdgcn_exp2f(z)); }
      g[T] = u32x2{pk2(e[0], e[1]), pk2(e[2], e[3])}; }
    const unsigned ho = gbase + G_HH + (((wv >> 1) * 56 + ((7 * j) >> 1)) * 2 + (wv & 1)) * 1024;
    if (j & 1) { stsc1_2(g[0], rS, lane * 16 + 8, ho);
      UNR for (int m2 = 0; m2 < 3; m2++) stsc1(u32x4{g[1 + 2 * m2][0], g[1 + 2 * m2][1], g[2 + 2 * m2][0], g[2 + 2 * m2][1]}, rS, lane * 16, ho + (m2 + 1) * 2048);
    } else {
      UNR for (int m2 = 0; m2 < 3; m2++) stsc1(u32x4{g[2 * m2][0], g[2 * m2][1], g[2 * m2 + 1][0], g[2 * m2 + 1][1]}, rS, lane * 16, ho + m2 * 2048);
      stsc1_2(g[6], rS, lane * 16, ho + 3 * 2048); }
  }
  STAMP(12);
  WAITV(4); W2CONV(4); W2LOAD(5, stg);
  BARRIER();
  const unsigned stg2 = wv * 7168;
  W2LOAD(6, stg2);
  { u32x4 hf[14]; const unsigned so = gbase + G_HH + (s * 56 + 7 * wv) * 2048;
    for (;;) { MEMFENCE(); unsigned m = 0;
      UNR for (int i = 0; i < 14; i++) hf[i] = ldsc1(rS, lane * 16, so + i * 1024);
      UNR for (int i = 0; i < 14; i++) m = vmax(hf[i], m);
      if (!anybad(m) || ++spins > SPIN_CAP) break; }
    STAMP(13);
    f32x4 acc[7][2];
    UNR for (int T = 0; T < 7; T++) { acc[T][0] = zero4; acc[T][1] = zero4; }
    UNR for (int kb = 0; kb < 7; kb++) UNR for (int T = 0; T < 7; T++) {
      const bf16x8 a = kb < 5 ? w2f[kb < 5 ? kb : 0][T] : trfrag((kb == 5 ? stg : stg2) + aI + 32 * T, (kb == 5 ? stg : stg2) + aI + 32 * T + 3584);
      acc[T][0] = mfma(a, asb(hf[2 * kb]), acc[T][0]); acc[T][1] = mfma(a, asb(hf[2 * kb + 1]), acc[T][1]); }
    MEMFENCE();
    UNR for (int u = 0; u < 14; u++) *(LAS f32x4*)((u < 7 ? stg + u * 1024 : stg2 + (u - 7) * 1024) + lane * 16) = acc[u >> 1][u & 1];
  }
  STAMP(14);
  BARRIER();
  if (wv < 7) {
    const unsigned u0 = 2 * wv, u1 = u0 + 1;
    const unsigned a0 = (u0 < 7 ? L_STG + u0 * 1024 : (u0 - 7) * 1024) + lane * 16, a1 = (u1 < 7 ? L_STG + u1 * 1024 : (u1 - 7) * 1024) + lane * 16;
    const f32x4 bb = {blo(b2r[0]), bhi(b2r[0]), blo(b2r[1]), bhi(b2r[1])};
    h0 += bb; h1 += bb;
    UNR for (int i = 0; i < 8; i++) { h0 += ldsf4(a0 + i * 7168); h1 += ldsf4(a1 + i * 7168); }
    GAS u16* o = (GAS u16*)out + (128 * t2 + 32 * s + r) * 448 + 112 * c + 16 * wv + 4 * q;
    *(GAS u32x2*)o = u32x2{pk2(h0[0], h0[1]), pk2(h0[2], h0[3])};
    *(GAS u32x2*)(o + 16 * 448) = u32x2{pk2(h1[0], h1[1]), pk2(h1[2], h1[3])};
  }
  WAITV(0);
  { const u64 tend = __builtin_amdgcn_s_memrealtime(); const float el = (float)(unsigned)(tend - tstart) * 0.01f;
    if (tid == 0 && el > THR) { GAS u16* o = (GAS u16*)out + (128 * t2 + 32 * s) * 448 + 112 * c; *o = (u16)(pk2(el, el) & 0xffff); } }
}

extern "C" void kernel_launch(void* const* d_in, const int* in_sizes, int n_in, void* d_out, int out_size, void* d_ws, size_t ws_size, hipStream_t stream) {
  (void)hipFuncSetAttribute((const void*)fused_kernel, hipFuncAttributeMaxDynamicSharedMemorySize, LDS_BYTES);
  fused_kernel<<<256, 512, LDS_BYTES, stream>>>((const u16*)d_in[0], (const u16*)d_in[1], (const u16*)d_in[2], (const u16*)d_in[3], (const u16*)d_in[5], (const u16*)d_in[6],
                                                (const u16*)d_in[7], (const u16*)d_in[8], (u16*)d_out, (char*)d_ws);
}
```

```cpp
#include <hip/hip_runtime.h>
#include <hip/hip_bf16.h>

typedef unsigned short u16;
typedef unsigned long long u64;
typedef __bf16 bf16x2 __attribute__((ext_vector_type(2)));
typedef __bf16 bf16x4 __attribute__((ext_vector_type(4)));
typedef __bf16 bf16x8 __attribute__((ext_vector_type(8)));
typedef float f32x2 __attribute__((ext_vector_type(2)));
typedef float f32x4 __attribute__((ext_vector_type(4)));
typedef unsigned u32x2 __attribute__((ext_vector_type(2)));
typedef unsigned u32x4 __attribute__((ext_vector_type(4)));
typedef __amdgpu_buffer_rsrc_t rsrc_t;
#define LAS __attribute__((address_space(3)))
#define GAS __attribute__((address_space(1)))
#define CAS __attribute__((address_space(4)))
#define DI __device__ __forceinline__
#define UNR _Pragma("unroll")

constexpr unsigned WS_FA1 = 4096, WS_FA2 = 8192, WS_FB = 12288;
constexpr unsigned WS_KV = 65536;
constexpr unsigned WS_O = WS_KV + (1u << 20);
constexpr unsigned WS_G = WS_O + (2u << 20);
constexpr unsigned G_H = 0, G_ST = 114688, G_HH = 118784, G_SZ = 589824;
constexpr unsigned WS_END = WS_G + 16 * G_SZ;
constexpr unsigned WS_DBG = 16u << 20;
constexpr unsigned L_WK = 0, L_WV = 28672, L_WQ = 57344, L_WO1 = 86016, L_PV = 130048, L_KVF = 158720, L_CS = 162816;
constexpr unsigned L_W1 = 0, L_OST = 65536, L_STG = 100352, LDS_BYTES = 163840;
constexpr unsigned SENT = 0xFFFFFFFFu;
constexpr int SPIN_CAP = 1 << 19;

DI unsigned rfl(unsigned v) { return __builtin_amdgcn_readfirstlane(v); }
DI rsrc_t mkrsrc(const void* p, unsigned bytes) { return __builtin_amdgcn_make_buffer_rsrc((void*)p, (short)0, (int)bytes, 0x00020000); }
DI bf16x4 trd(unsigned a) { return __builtin_amdgcn_ds_read_tr16_b64_v4bf16((LAS bf16x4*)a); }
DI bf16x8 cat(bf16x4 a, bf16x4 b) { return __builtin_shufflevector(a, b, 0, 1, 2, 3, 4, 5, 6, 7); }
DI bf16x8 trfrag(unsigned a0, unsigned a1) { return cat(trd(a0), trd(a1)); }
DI f32x4 mfma(bf16x8 a, bf16x8 b, f32x4 c) { return __builtin_amdgcn_mfma_f32_16x16x32_bf16(a, b, c, 0, 0, 0); }
DI unsigned pk2(float a, float b) { f32x2 f = {a, b}; return __builtin_bit_cast(unsigned, __builtin_convertvector(f, bf16x2)); }
DI bf16x8 asb(u32x4 v) { return __builtin_bit_cast(bf16x8, v); }
DI float blo(unsigned u) { return __builtin_bit_cast(float, u << 16); }
DI float bhi(unsigned u) { return __builtin_bit_cast(float, u & 0xffff0000u); }
DI unsigned f2u(float f) { return __builtin_bit_cast(unsigned, f); }
DI float u2f(unsigned u) { return __builtin_bit_cast(float, u); }
DI float allq_sum(float v) {
  auto a = __builtin_amdgcn_permlane32_swap(f2u(v), f2u(v), false, false); float t = u2f(a[0]) + u2f(a[1]);
  auto b = __builtin_amdgcn_permlane16_swap(f2u(t), f2u(t), false, false); return u2f(b[0]) + u2f(b[1]);
}
DI float allq_max(float v) {
  auto a = __builtin_amdgcn_permlane32_swap(f2u(v), f2u(v), false, false); float t = fmaxf(u2f(a[0]), u2f(a[1]));
  auto b = __builtin_amdgcn_permlane16_swap(f2u(t), f2u(t), false, false); return fmaxf(u2f(b[0]), u2f(b[1]));
}
DI float bcast_q3(float v) {
  auto a = __builtin_amdgcn_permlane32_swap(f2u(v), f2u(v), false, false);
  auto b = __builtin_amdgcn_permlane16_swap(a[1], a[1], false, false); return u2f(b[1]);
}
DI unsigned umax3(unsigned a, unsigned b, unsigned c) { return max(max(a, b), c); }
DI unsigned vmax(u32x4 v, unsigned m) { return umax3(umax3(v.x, v.y, v.z), v.w, m); }
DI bool anybad(unsigned m) { return __builtin_amdgcn_ballot_w64(m == SENT) != 0; }
#define WAITV(n) asm volatile("s_waitcnt vmcnt(%0)" :: "i"(n) : "memory")
#define BARRIER() asm volatile("s_waitcnt lgkmcnt(0)\n\ts_barrier" ::: "memory")
#define MEMFENCE() asm volatile("" ::: "memory")
DI void glds(unsigned ldsa, unsigned voff, rsrc_t rs, unsigned soff) {
  asm volatile("s_mov_b32 m0, %0\n\ts_nop 0\n\tbuffer_load_dwordx4 %1, %2, %3 offen lds" :: "s"(ldsa), "v"(voff), "s"(rs), "s"(soff) : "memory");
}
DI void glds_nt(unsigned ldsa, unsigned voff, rsrc_t rs, unsigned soff) {
  asm volatile("s_mov_b32 m0, %0\n\ts_nop 0\n\tbuffer_load_dwordx4 %1, %2, %3 offen nt lds" :: "s"(ldsa), "v"(voff), "s"(rs), "s"(soff) : "memory");
}
template <unsigned RS> DI unsigned img_voff(unsigned i, unsigned lane) { unsigned n = i * 64 + lane, row = (n * 9363u) >> 17; return row * RS + (n - row * 14u) * 16u; }
DI u32x4 ldsc1(rsrc_t rs, unsigned voff, unsigned soff) { return __builtin_amdgcn_raw_buffer_load_b128(rs, voff, soff, 16); }
DI void stsc1(u32x4 v, rsrc_t rs, unsigned voff, unsigned soff) { __builtin_amdgcn_raw_buffer_store_b128(v, rs, voff, soff, 16); asm volatile("s_nop 1" :: "v"(v)); }
DI void stsc1_2(u32x2 v, rsrc_t rs, unsigned voff, unsigned soff) { __builtin_amdgcn_raw_buffer_store_b64(v, rs, voff, soff, 16); }
DI f32x4 ldsf4(unsigned a) { return *(LAS f32x4*)a; }
DI u32x4 ldsu4(unsigned a) { return *(LAS u32x4*)a; }

#define NSTAMP 16
#define STAMP(k) do {} while (0)
#define CSTAMP(k) do {} while (0)

DI void featmap(const f32x4 a0, const f32x4 a1, const bf16x8 ph0, const bf16x8 ph1, const bool pad, float (&ep)[8], float (&en)[8]) {
  u32x4 kb = {pk2(a0[0], a0[1]), pk2(a0[2], a0[3]), pad ? 0u : pk2(a1[0], a1[1]), pad ? 0u : pk2(a1[2], a1[3])};
  const f32x4 z = {0.f, 0.f, 0.f, 0.f};
  f32x4 p0 = mfma(ph0, asb(kb), z), p1 = mfma(ph1, asb(kb), z);
  float p[8];
  UNR for (int i = 0; i < 4; i++) { p[i] = p0[i]; p[4 + i] = pad ? p0[i] : p1[i]; }
  float mx = fmaxf(fmaxf(fmaxf(p[0], p[1]), fmaxf(p[2], p[3])), fmaxf(fmaxf(p[4], p[5]), fmaxf(p[6], p[7])));
  float mn = fminf(fminf(fminf(p[0], p[1]), fminf(p[2], p[3])), fminf(fminf(p[4], p[5]), fminf(p[6], p[7])));
  mx = allq_max(mx); mn = -allq_max(-mn);
  const float L2E = 1.4426950408889634f;
  const float cx = -mx * L2E, cn = mn * L2E;
  float sp = 0.f, sn = 0.f;
  UNR for (int i = 0; i < 8; i++) {
    float e1 = __builtin_amdgcn_exp2f(fmaf(p[i], L2E, cx)), e2 = __builtin_amdgcn_exp2f(fmaf(p[i], -L2E, cn));
    if (i >= 4) { e1 = pad ? 0.f : e1; e2 = pad ? 0.f : e2; }
    ep[i] = e1; en[i] = e2; sp += e1; sn += e2;
  }
  sp = __builtin_amdgcn_rcpf(allq_sum(sp)); sn = __builtin_amdgcn_rcpf(allq_sum(sn));
  UNR for (int i = 0; i < 8; i++) { ep[i] *= sp; en[i] *= sn; }
}

__global__ void __launch_bounds__(512, 2) fused_kernel(const u16* __restrict__ x, const u16* __restrict__ wqkv, const u16* __restrict__ wphi, const u16* __restrict__ wo,
                                                       const u16* __restrict__ w1, const u16* __restrict__ b1, const u16* __restrict__ w2, const u16* __restrict__ b2,
                                                       u16* __restrict__ out, char* __restrict__ ws) {
  const unsigned tid = threadIdx.x, lane = tid & 63, wv = rfl(tid >> 6);
  const unsigned q = lane >> 4, r = lane & 15, qq = (lane >> 2) & 3, p = lane & 3;
  const unsigned w = blockIdx.x, x8 = w & 7, ii = w >> 3;
  const unsigned t1 = 4 * (x8 >> 1) + (ii >> 3), hd = 8 * (x8 & 1) + (ii & 7);
  const unsigned t2 = 2 * x8 + (ii >> 4), j = ii & 15, s = j >> 2, c = j & 3;
  const bool pad = (q == 3);
  u64 tag = *(const CAS u64*)ws + 1ull;
  asm volatile("" : "+s"(tag));
  const rsrc_t rX = mkrsrc(x, 2048 * 896), rW = mkrsrc(wqkv, 448 * 2688), rP = mkrsrc(wphi, 16 * 1568), rWO = mkrsrc(wo, 448 * 896);
  const rsrc_t rW1 = mkrsrc(w1, 448 * 3584), rW2 = mkrsrc(w2, 1792 * 896), rS = mkrsrc(ws, WS_END);
  const unsigned gbase = WS_G + t2 * G_SZ;
  const u32x4 sent4 = {SENT, SENT, SENT, SENT};
  const f32x4 zero4 = {0.f, 0.f, 0.f, 0.f};
  const u32x4 ones_u = {0x3f803f80u, 0x3f803f80u, 0x3f803f80u, 0x3f803f80u};
  const bf16x8 ones = asb(ones_u);
  int spins = 0;
  STAMP(0); CSTAMP(30);

  const unsigned kvslot = WS_KV + (hd * 16 + t1) * 4096 + wv * 512, oslot = WS_O + (((t1 * 4 + (wv >> 1)) * 16 + hd) * 2 + (wv & 1)) * 1024;
  stsc1_2(u32x2{SENT, SENT}, rS, lane * 8, kvslot);
  stsc1(sent4, rS, lane * 16, oslot);
  if (wv < 2) glds(L_KVF + wv * 1024, lane * 16, rP, hd * 1568 + wv * 1024);
  const unsigned wvoff = (lane >> 2) * 2688 + (((lane & 3) ^ ((lane >> 5) << 1)) << 4), wwin = (56 * hd) & ~15u;
  UNR for (int m = 0; m < 7; m++) { const unsigned n = wv + 8 * m, sec = n >= 28; glds(n * 1024, wvoff, rW, (n - 28 * sec) * (16 * 2688) + (sec + 1) * 896 + wwin); }
  u32x4 xf[14];
  { const unsigned xv = (128 * t1 + 16 * wv + r) * 896 + q * 16;
    UNR for (int ks = 0; ks < 14; ks++) asm volatile("buffer_load_dwordx4 %0, %1, %2, 0 offen offset:%3" : "=v"(xf[ks]) : "v"(xv), "s"(rX), "i"(ks * 64)); }
  UNR for (int m = 0; m < 4; m++) { const unsigned n = min(wv + 8 * m, 27u); glds(L_WQ + n * 1024, wvoff, rW, n * (16 * 2688) + wwin); }
  UNR for (int m = 0; m < 5; m++) { const unsigned n = wv + 8 * m; stsc1(sent4, rS, lane * 16, n < 36 ? gbase + (j * 36 + n) * 1024 : 0xF0000000u); }
  UNR for (int m = 0; m < 6; m++) { const unsigned n = wv + 8 * m; if (m < 5 || n < 43) glds(L_WO1 + n * 1024, img_voff<896>(n, lane), rWO, 224 * c); }
  constexpr int NY = 14;
  WAITV(14 + NY);
  BARRIER();
  STAMP(1);
  if (wv == 7 && lane < 2) __hip_atomic_store((GAS u64*)(ws + (lane ? WS_FA2 + (t1 * 16 + hd) * 8 : WS_FA1 + (hd * 16 + t1) * 8)), tag, __ATOMIC_RELAXED, __HIP_MEMORY_SCOPE_AGENT);
  bf16x8 ph0, ph1;
  { const unsigned r0 = (4 * q + qq) * 56, r1 = (pad ? 4 * q + qq : 16 + 4 * q + qq) * 56;
    ph0 = trfrag(L_KVF + r0 + 8 * p, L_KVF + r1 + 8 * p); ph1 = trfrag(L_KVF + r0 + 32 + 8 * p, L_KVF + r1 + 32 + 8 * p); }
  const unsigned sh = 8 * (hd & 1), wrow = (8 * q + qq) * 64;
  const unsigned aW0 = wrow + ((8 * p + sh) ^ ((q & 1) << 5)), aW1 = wrow + (min(32 + 8 * p + sh, 56u) ^ ((q & 1) << 5));
  if (wv < 4) {
    const unsigned a = ((wv & 1) ? aW1 : aW0) + (wv >> 1) * 28672; f32x4 cs = zero4;
    UNR for (int ks = 0; ks < 14; ks++) cs = mfma(trfrag(a + ks * 2048, a + ks * 2048 + 256), ones, cs);
    if (r == 0) *(LAS f32x4*)(L_CS + wv * 64 + q * 16) = cs;
  }
  f32x4 aK0 = zero4, aK1 = zero4, aV0 = zero4, aV1 = zero4, aS = zero4, aQ = zero4;
  UNR for (int ks = 0; ks < 14; ks++) {
    asm volatile("s_waitcnt vmcnt(%1)" : "+v"(xf[ks]) : "i"(13 - ks + NY));
    const bf16x8 xb = asb(xf[ks]);
    aK0 = mfma(trfrag(aW0 + L_WK + ks * 2048, aW0 + L_WK + ks * 2048 + 256), xb, aK0);
    aK1 = mfma(trfrag(aW1 + L_WK + ks * 2048, aW1 + L_WK + ks * 2048 + 256), xb, aK1);
    aV0 = mfma(trfrag(aW0 + L_WV + ks * 2048, aW0 + L_WV + ks * 2048 + 256), xb, aV0);
    aV1 = mfma(trfrag(aW1 + L_WV + ks * 2048, aW1 + L_WV + ks * 2048 + 256), xb, aV1);
    aS = mfma(ones, xb, aS); aQ = mfma(xb, xb, aQ);
  }
  float mean, rstd;
  { const unsigned ri = r & 3; float d = ri == 0 ? aQ[0] : ri == 1 ? aQ[1] : ri == 2 ? aQ[2] : aQ[3]; d = (q == (r >> 2)) ? d : 0.f;
    const float sq = allq_sum(d); mean = aS[0] * (1.f / 448.f); const float var = fmaxf(sq * (1.f / 448.f) - mean * mean, 0.f); rstd = __builtin_amdgcn_rsqf(var + 1e-5f); }
  WAITV(10);
  BARRIER();
  STAMP(2);
  { const float nm = -mean * rstd;
    const f32x4 c0 = ldsf4(L_CS + q * 16), c1 = ldsf4(L_CS + 64 + q * 16), c2 = ldsf4(L_CS + 128 + q * 16), c3 = ldsf4(L_CS + 192 + q * 16);
    UNR for (int i = 0; i < 4; i++) { aK0[i] = fmaf(aK0[i], rstd, nm * c0[i]); aK1[i] = fmaf(aK1[i], rstd, nm * c1[i]); aV0[i] = fmaf(aV0[i], rstd, nm * c2[i]); aV1[i] = fmaf(aV1[i], rstd, nm * c3[i]); }
    float ep[8], en[8];
    featmap(aK0, aK1, ph0, ph1, pad, ep, en);
    const unsigned a = L_PV + (16 * wv + r) * 224 + ((q ^ (r >> 2)) << 3);
    *(LAS u32x2*)(a) = u32x2{pk2(ep[0], ep[1]), pk2(ep[2], ep[3])};
    *(LAS u32x2*)(a + 32) = u32x2{pk2(ep[4], ep[5]), pk2(ep[6], ep[7])};
    *(LAS u32x2*)(a + 64) = u32x2{pk2(en[0], en[1]), pk2(en[2], en[3])};
    *(LAS u32x2*)(a + 96) = u32x2{pk2(en[4], en[5]), pk2(en[6], en[7])};
    *(LAS u32x2*)(a + 128) = u32x2{pk2(aV0[0], aV0[1]), pk2(aV0[2], aV0[3])};
    *(LAS u32x2*)(a + 160) = u32x2{pad ? 0x00003f80u : pk2(aV1[0], aV1[1]), pad ? 0u : pk2(aV1[2], aV1[3])};
  }
  BARRIER();
  { const unsigned ab = L_PV + (4 * q + qq) * 224 + ((p ^ q) << 3), aA = ab + (2 * (wv >> 2) + (wv & 1)) * 32, aB = ab + (4 + ((wv >> 1) & 1)) * 32;
    f32x4 kv = zero4;
    UNR for (int kk = 0; kk < 4; kk++) kv = mfma(trfrag(aA + kk * 7168, aA + kk * 7168 + 3584), trfrag(aB + kk * 7168, aB + kk * 7168 + 3584), kv);
    stsc1_2(u32x2{pk2(kv[0], kv[1]), pk2(kv[2], kv[3])}, rS, lane * 8, kvslot);
  }
  STAMP(3);
  UNR for (int m = 0; m < 7; m++) { const unsigned n = wv + 8 * m; glds(n * 1024, img_voff<896>(n, lane), rWO, 196 * 896 + 224 * c); }
  bf16x8 phq0, phq1;
  { f32x4 aQ0 = zero4, aQ1 = zero4;
    const unsigned a0 = aW0 + L_WQ, a1 = aW1 + L_WQ;
    if (wv >= 6) { const unsigned a = (wv & 1) ? a1 : a0; f32x4 cs = zero4;
      UNR for (int ks = 0; ks < 14; ks++) cs = mfma(trfrag(a + ks * 2048, a + ks * 2048 + 256), ones, cs);
      if (r == 0) *(LAS f32x4*)(L_CS + 256 + (wv & 1) * 64 + q * 16) = cs; }
    UNR for (int ks = 0; ks < 14; ks++) { const bf16x8 xb = asb(xf[ks]);
      aQ0 = mfma(trfrag(a0 + ks * 2048, a0 + ks * 2048 + 256), xb, aQ0); aQ1 = mfma(trfrag(a1 + ks * 2048, a1 + ks * 2048 + 256), xb, aQ1); }
    BARRIER();
    const float nm = -mean * rstd; const f32x4 c0 = ldsf4(L_CS + 256 + q * 16), c1 = ldsf4(L_CS + 320 + q * 16);
    UNR for (int i = 0; i < 4; i++) { aQ0[i] = fmaf(aQ0[i], rstd, nm * c0[i]); aQ1[i] = fmaf(aQ1[i], rstd, nm * c1[i]); }
    float ep[8], en[8];
    featmap(aQ0, aQ1, ph0, ph1, pad, ep, en);
    phq0 = asb(u32x4{pk2(ep[0], ep[1]), pk2(ep[2], ep[3]), pk2(ep[4], ep[5]), pk2(ep[6], ep[7])});
    phq1 = asb(u32x4{pk2(en[0], en[1]), pk2(en[2], en[3]), pk2(en[4], en[5]), pk2(en[6], en[7])});
  }
  if (wv == 7) {
    const unsigned fo = lane < 16 ? WS_FA1 + (hd * 16 + lane) * 8 : WS_FA2 + (t2 * 16 + (lane & 15)) * 8;
    for (;;) { MEMFENCE(); const u64 f = __hip_atomic_load((GAS u64*)(ws + fo), __ATOMIC_RELAXED, __HIP_MEMORY_SCOPE_AGENT);
      if (__builtin_amdgcn_ballot_w64(f != tag) == 0 || ++spins > SPIN_CAP) break; }
  }
  WAITV(0);
  BARRIER();
  STAMP(4);
  if (wv == 7 && lane == 0) __hip_atomic_store((GAS u64*)(ws + WS_FB + (t2 * 16 + j) * 8), tag, __ATOMIC_RELAXED, __HIP_MEMORY_SCOPE_AGENT);
  const unsigned Tw = min(wv, 6u);
  const unsigned aI = (4 * q + qq) * 224 + 8 * p;
  bf16x8 wof[16];
  { const unsigned a0 = aI + 32 * Tw, a1 = a0 + (pad ? 0u : 3584u);
    UNR for (int kk = 0; kk < 16; kk++) { const unsigned b = kk < 7 ? L_WO1 + kk * 6272 : (kk - 7) * 6272; wof[kk] = trfrag(a0 + b, a1 + b); } }
  { u32x4 v[8]; const unsigned vo = (lane >> 5) * 8 * 4096 + (lane & 31) * 16, so = WS_KV + hd * 16 * 4096 + wv * 512;
    for (;;) { MEMFENCE(); unsigned m = 0;
      UNR for (int i = 0; i < 8; i++) v[i] = ldsc1(rS, vo, so + i * 4096);
      UNR for (int i = 0; i < 8; i++) m = vmax(v[i], m);
      if (!anybad(m) || ++spins > SPIN_CAP) break; }
    float a[8];
    UNR for (int e = 0; e < 4; e++) { a[2 * e] = blo(v[0][e]); a[2 * e + 1] = bhi(v[0][e]); }
    UNR for (int i = 1; i < 8; i++) UNR for (int e = 0; e < 4; e++) { a[2 * e] += blo(v[i][e]); a[2 * e + 1] += bhi(v[i][e]); }
    float b[4];
    UNR for (int e = 0; e < 4; e++) { auto sw = __builtin_amdgcn_permlane32_swap(f2u(a[e]), f2u(a[4 + e]), false, false); b[e] = u2f(sw[0]) + u2f(sw[1]); }
    *(LAS u32x2*)(L_KVF + (wv >> 1) * 1024 + (2 * (lane & 31) + (lane >> 5)) * 16 + (wv & 1) * 8) = u32x2{pk2(b[0], b[1]), pk2(b[2], b[3])};
  }
  BARRIER();
  STAMP(5);
  { const bf16x8 k0 = asb(ldsu4(L_KVF + lane * 16)), k1 = asb(ldsu4(L_KVF + 1024 + lane * 16)), k2 = asb(ldsu4(L_KVF + 2048 + lane * 16)), k3 = asb(ldsu4(L_KVF + 3072 + lane * 16));
    f32x4 o0 = mfma(k0, phq0, zero4), o1 = mfma(k1, phq0, zero4); o0 = mfma(k2, phq1, o0); o1 = mfma(k3, phq1, o1);
    const float rd = __builtin_amdgcn_rcpf(bcast_q3(o1[0]) + 1e-6f);
    stsc1(u32x4{pk2(o0[0] * rd, o0[1] * rd), pk2(o0[2] * rd, o0[3] * rd), pad ? 0u : pk2(o1[0] * rd, o1[1] * rd), pad ? 0u : pk2(o1[2] * rd, o1[3] * rd)}, rS, lane * 16, oslot);
  }
  STAMP(6);
  UNR for (int m = 0; m < 8; m++) { const unsigned n = wv + 8 * m; if (m < 7 || n < 63) glds(L_W1 + n * 1024, img_voff<3584>(n, lane), rW1, 224 * j); }
  const unsigned stg = L_STG + wv * 7168, w2s = (224 * wv) * 896 + 224 * c;
  UNR for (int m = 0; m < 7; m++) glds(stg + m * 1024, img_voff<896>(m, lane), rW2, w2s);
  u32x2 xr0, xr1, b1r, b2r;
  { const unsigned xo = (128 * t2 + 32 * s + r) * 896 + 224 * c + 32 * Tw + 8 * q;
    xr0 = __builtin_amdgcn_raw_buffer_load_b64(rX, xo, 0, 0); xr1 = __builtin_amdgcn_raw_buffer_load_b64(rX, xo, 16 * 896, 0);
    b1r = *(const GAS u32x2*)(b1 + 112 * j + 16 * Tw + 4 * q); b2r = *(const GAS u32x2*)(b2 + 112 * c + 16 * Tw + 4 * q); }
  if (wv == 7) {
    const unsigned fo = WS_FB + (t2 * 16 + r) * 8;
    for (;;) { MEMFENCE(); const u64 f = __hip_atomic_load((GAS u64*)(ws + fo), __ATOMIC_RELAXED, __HIP_MEMORY_SCOPE_AGENT);
      if (__builtin_amdgcn_ballot_w64(f != tag) == 0 || ++spins > SPIN_CAP) break; }
  }
  { u32x4 v[4]; const unsigned so = WS_O + ((t2 * 4 + s) * 16 + 2 * wv) * 2048;
    for (;;) { MEMFENCE(); unsigned m = 0;
      UNR for (int i = 0; i < 4; i++) v[i] = ldsc1(rS, lane * 16, so + i * 1024);
      UNR for (int i = 0; i < 4; i++) m = vmax(v[i], m);
      if (!anybad(m) || ++spins > SPIN_CAP) break; }
    UNR for (int i = 0; i < 4; i++) *(LAS u32x4*)(L_OST + (4 * wv + i) * 1024 + lane * 16) = v[i];
  }
  BARRIER();
  STAMP(7);
  if (w == 0 && wv == 7 && lane == 0) __hip_atomic_store((GAS u64*)ws, tag, __ATOMIC_RELAXED, __HIP_MEMORY_SCOPE_AGENT);
  f32x4 h0 = zero4, h1 = zero4;
  if (wv < 7) {
    UNR for (int kk = 0; kk < 16; kk++) { h0 = mfma(wof[kk], asb(ldsu4(L_OST + kk * 2048 + lane * 16)), h0); h1 = mfma(wof[kk], asb(ldsu4(L_OST + kk * 2048 + 1024 + lane * 16)), h1); }
    h0[0] += blo(xr0[0]); h0[1] += bhi(xr0[0]); h0[2] += blo(xr0[1]); h0[3] += bhi(xr0[1]);
    h1[0] += blo(xr1[0]); h1[1] += bhi(xr1[0]); h1[2] += blo(xr1[1]); h1[3] += bhi(xr1[1]);
    const unsigned TT = 7 * c + wv, so = gbase + G_H + (2 * s * 14 + (TT >> 1)) * 1024 + (TT & 1) * 8;
    stsc1_2(u32x2{pk2(h0[0], h0[1]), pk2(h0[2], h0[3])}, rS, lane * 16, so);
    stsc1_2(u32x2{pk2(h1[0], h1[1]), pk2(h1[2], h1[3])}, rS, lane * 16, so + 14 * 1024);
    const float s0 = allq_sum((h0[0] + h0[1]) + (h0[2] + h0[3])), s1 = allq_sum((h1[0] + h1[1]) + (h1[2] + h1[3]));
    const float q0 = allq_sum(h0[0] * h0[0] + h0[1] * h0[1] + h0[2] * h0[2] + h0[3] * h0[3]), q1 = allq_sum(h1[0] * h1[0] + h1[1] * h1[1] + h1[2] * h1[2] + h1[3] * h1[3]);
    if (q < 2) *(LAS f32x2*)(L_KVF + wv * 256 + lane * 8) = q ? f32x2{s1, q1} : f32x2{s0, q0};
  }
  STAMP(8);
  BARRIER();
  if (wv == 7 && lane < 32) { f32x2 a = *(LAS f32x2*)(L_KVF + lane * 8);
    UNR for (int i = 1; i < 7; i++) a += *(LAS f32x2*)(L_KVF + i * 256 + lane * 8);
    stsc1_2(u32x2{f2u(a[0]), f2u(a[1])}, rS, (lane >> 4) * 512 + (lane & 15) * 8, gbase + G_ST + 2 * s * 512 + c * 128); }
  UNR for (int m = 0; m < 5; m++) { const unsigned n = 63 + wv + 8 * m; if (m < 4 || n < 98) glds(L_W1 + n * 1024, img_voff<3584>(n, lane), rW1, 224 * j); }
  bf16x8 w2f[5][7];
#define W2CONV(B) do { UNR for (int T = 0; T < 7; T++) w2f[B][T] = trfrag(stg + aI + 32 * T, stg + aI + 32 * T + 3584); asm volatile("s_waitcnt lgkmcnt(0)" ::: "memory"); \
    UNR for (int T = 0; T < 7; T++) asm volatile("" : "+v"(w2f[B][T])); } while (0)
#define W2LOAD(B, DST) do { UNR for (int m = 0; m < 7; m++) glds((DST) + m * 1024, img_voff<896>(m, lane), rW2, w2s + (B) * 32 * 896); } while (0)
  W2CONV(0);
  W2LOAD(1, stg);
  WAITV(7);
  BARRIER();
  if (wv < 7) { f32x4 cs = zero4; const unsigned a = L_W1 + aI + 32 * wv;
    UNR for (int ks = 0; ks < 14; ks++) cs = mfma(trfrag(a + ks * 7168, a + ks * 7168 + 3584), ones, cs);
    if (r == 0) { *(LAS f32x4*)(L_CS + wv * 128 + q * 32) = cs; *(LAS f32x4*)(L_CS + wv * 128 + q * 32 + 16) = f32x4{blo(b1r[0]), bhi(b1r[0]), blo(b1r[1]), bhi(b1r[1])}; } }
  WAITV(0); W2CONV(1); W2LOAD(2, stg);
  BARRIER();
  STAMP(9);
  { u32x4 hf[14]; u32x2 st; const unsigned so = gbase + G_H + wv * 14 * 1024;
    for (;;) { MEMFENCE(); unsigned m = 0;
      UNR for (int i = 0; i < 14; i++) hf[i] = ldsc1(rS, lane * 16, so + i * 1024);
      UNR for (int i = 0; i < 14; i++) m = vmax(hf[i], m);
      if (!anybad(m) || ++spins > SPIN_CAP) break; }
    STAMP(10);
    W2CONV(2); W2LOAD(3, stg);
    f32x4 acc[7];
    UNR for (int T = 0; T < 7; T++) acc[T] = zero4;
    UNR for (int ks = 0; ks < 14; ks++) UNR for (int T = 0; T < 7; T++)
      acc[T] = mfma(trfrag(L_W1 + aI + ks * 7168 + 32 * T, L_W1 + aI + ks * 7168 + 32 * T + 3584), asb(hf[ks]), acc[T]);
    STAMP(11);
    for (;;) { MEMFENCE(); st = __builtin_amdgcn_raw_buffer_load_b64(rS, lane * 8, gbase + G_ST + wv * 512, 16);
      if (!anybad(max(st[0], st[1])) || ++spins > SPIN_CAP) break; }
    W2CONV(3); W2LOAD(4, stg);
    const float m = allq_sum(u2f(st[0])) * (1.f / 448.f), var = fmaxf(allq_sum(u2f(st[1])) * (1.f / 448.f) - m * m, 0.f);
    const float r1 = __builtin_amdgcn_rsqf(var + 1e-5f), R = r1 * __builtin_amdgcn_rsqf(var * r1 * r1 + 1e-5f), nm = -m * R;
    u32x2 g[7];
    UNR for (int T = 0; T < 7; T++) { const f32x4 cs = ldsf4(L_CS + T * 128 + q * 32), bb = ldsf4(L_CS + T * 128 + q * 32 + 16); float e[4];
      UNR for (int i = 0; i < 4; i++) { const float v = fmaf(acc[T][i], R, fmaf(nm, cs[i], bb[i]));
        const float C1 = -2.302208198f, C2 = C1 * 0.044715f;
        const float z = v * fmaf(v * v, C2, C1); e[i] = v * __builtin_amdgcn_rcpf(1.f + __builtin_amdgcn_exp2f(z)); }
      g[T] = u32x2{pk2(e[0], e[1]), pk2(e[2], e[3])}; }
    const unsigned ho = gbase + G_HH + (((wv >> 1) * 56 + ((7 * j) >> 1)) * 2 + (wv & 1)) * 1024;
    if (j & 1) { stsc1_2(g[0], rS, lane * 16 + 8, ho);
      UNR for (int m2 = 0; m2 < 3; m2++) stsc1(u32x4{g[1 + 2 * m2][0], g[1 + 2 * m2][1], g[2 + 2 * m2][0], g[2 + 2 * m2][1]}, rS, lane * 16, ho + (m2 + 1) * 2048);
    } else {
      UNR for (int m2 = 0; m2 < 3; m2++) stsc1(u32x4{g[2 * m2][0], g[2 * m2][1], g[2 * m2 + 1][0], g[2 * m2 + 1][1]}, rS, lane * 16, ho + m2 * 2048);
      stsc1_2(g[6], rS, lane * 16, ho + 3 * 2048); }
  }
  STAMP(12);
  WAITV(4); W2CONV(4); W2LOAD(5, stg);
  BARRIER();
  const unsigned stg2 = wv * 7168;
  W2LOAD(6, stg2);
  { u32x4 hf[14]; const unsigned so = gbase + G_HH + (s * 56 + 7 * wv) * 2048;
    for (;;) { MEMFENCE(); unsigned m = 0;
      UNR for (int i = 0; i < 14; i++) hf[i] = ldsc1(rS, lane * 16, so + i * 1024);
      UNR for (int i = 0; i < 14; i++) m = vmax(hf[i], m);
      if (!anybad(m) || ++spins > SPIN_CAP) break; }
    STAMP(13);
    f32x4 acc[7][2];
    UNR for (int T = 0; T < 7; T++) { acc[T][0] = zero4; acc[T][1] = zero4; }
    UNR for (int kb = 0; kb < 7; kb++) UNR for (int T = 0; T < 7; T++) {
      const bf16x8 a = kb < 5 ? w2f[kb < 5 ? kb : 0][T] : trfrag((kb == 5 ? stg : stg2) + aI + 32 * T, (kb == 5 ? stg : stg2) + aI + 32 * T + 3584);
      acc[T][0] = mfma(a, asb(hf[2 * kb]), acc[T][0]); acc[T][1] = mfma(a, asb(hf[2 * kb + 1]), acc[T][1]); }
    MEMFENCE();
    UNR for (int u = 0; u < 14; u++) *(LAS f32x4*)((u < 7 ? stg + u * 1024 : stg2 + (u - 7) * 1024) + lane * 16) = acc[u >> 1][u & 1];
  }
  STAMP(14);
  BARRIER();
  if (wv < 7) {
    const unsigned u0 = 2 * wv, u1 = u0 + 1;
    const unsigned a0 = (u0 < 7 ? L_STG + u0 * 1024 : (u0 - 7) * 1024) + lane * 16, a1 = (u1 < 7 ? L_STG + u1 * 1024 : (u1 - 7) * 1024) + lane * 16;
    const f32x4 bb = {blo(b2r[0]), bhi(b2r[0]), blo(b2r[1]), bhi(b2r[1])};
    h0 += bb; h1 += bb;
    UNR for (int i = 0; i < 8; i++) { h0 += ldsf4(a0 + i * 7168); h1 += ldsf4(a1 + i * 7168); }
    GAS u16* o = (GAS u16*)out + (128 * t2 + 32 * s + r) * 448 + 112 * c + 16 * wv + 4 * q;
    *(GAS u32x2*)o = u32x2{pk2(h0[0], h0[1]), pk2(h0[2], h0[3])};
    *(GAS u32x2*)(o + 16 * 448) = u32x2{pk2(h1[0], h1[1]), pk2(h1[2], h1[3])};
  }
  STAMP(15); CSTAMP(31);
}

extern "C" void kernel_launch(void* const* d_in, const int* in_sizes, int n_in, void* d_out, int out_size, void* d_ws, size_t ws_size, hipStream_t stream) {
  (void)hipFuncSetAttribute((const void*)fused_kernel, hipFuncAttributeMaxDynamicSharedMemorySize, LDS_BYTES);
  fused_kernel<<<256, 512, LDS_BYTES, stream>>>((const u16*)d_in[0], (const u16*)d_in[1], (const u16*)d_in[2], (const u16*)d_in[3], (const u16*)d_in[5], (const u16*)d_in[6],
                                                (const u16*)d_in[7], (const u16*)d_in[8], (u16*)d_out, (char*)d_ws);
}
```

```cpp
#define MODE 0
#define THR -1.f
#include <hip/hip_runtime.h>
#include <hip/hip_bf16.h>

typedef unsigned short u16;
typedef unsigned long long u64;
typedef __bf16 bf16x2 __attribute__((ext_vector_type(2)));
typedef __bf16 bf16x4 __attribute__((ext_vector_type(4)));
typedef __bf16 bf16x8 __attribute__((ext_vector_type(8)));
typedef float f32x2 __attribute__((ext_vector_type(2)));
typedef float f32x4 __attribute__((ext_vector_type(4)));
typedef unsigned u32x2 __attribute__((ext_vector_type(2)));
typedef unsigned u32x4 __attribute__((ext_vector_type(4)));
typedef __amdgpu_buffer_rsrc_t rsrc_t;
#define LAS __attribute__((address_space(3)))
#define GAS __attribute__((address_space(1)))
#define CAS __attribute__((address_space(4)))
#define DI __device__ __forceinline__
#define UNR _Pragma("unroll")

constexpr unsigned WS_FA1 = 4096, WS_FA2 = 8192, WS_FB = 12288;
constexpr unsigned WS_KV = 65536;
constexpr unsigned WS_O = WS_KV + (1u << 20);
constexpr unsigned WS_G = WS_O + (2u << 20);
constexpr unsigned G_H = 0, G_ST = 114688, G_HH = 118784, G_SZ = 589824;
constexpr unsigned WS_END = WS_G + 16 * G_SZ;
constexpr unsigned WS_DBG = 16u << 20;
constexpr unsigned L_WK = 0, L_WV = 28672, L_WQ = 57344, L_WO1 = 86016, L_PV = 130048, L_KVF = 158720, L_CS = 162816;
constexpr unsigned L_W1 = 0, L_OST = 65536, L_STG = 100352, LDS_BYTES = 163840;
constexpr unsigned SENT = 0xFFFFFFFFu;
constexpr int SPIN_CAP = 1 << 19;

DI unsigned rfl(unsigned v) { return __builtin_amdgcn_readfirstlane(v); }
DI rsrc_t mkrsrc(const void* p, unsigned bytes) { return __builtin_amdgcn_make_buffer_rsrc((void*)p, (short)0, (int)bytes, 0x00020000); }
DI bf16x4 trd(unsigned a) { return __builtin_amdgcn_ds_read_tr16_b64_v4bf16((LAS bf16x4*)a); }
DI bf16x8 cat(bf16x4 a, bf16x4 b) { return __builtin_shufflevector(a, b, 0, 1, 2, 3, 4, 5, 6, 7); }
DI bf16x8 trfrag(unsigned a0, unsigned a1) { return cat(trd(a0), trd(a1)); }
DI f32x4 mfma(bf16x8 a, bf16x8 b, f32x4 c) { return __builtin_amdgcn_mfma_f32_16x16x32_bf16(a, b, c, 0, 0, 0); }
DI unsigned pk2(float a, float b) { f32x2 f = {a, b}; return __builtin_bit_cast(unsigned, __builtin_convertvector(f, bf16x2)); }
DI bf16x8 asb(u32x4 v) { return __builtin_bit_cast(bf16x8, v); }
DI float blo(unsigned u) { return __builtin_bit_cast(float, u << 16); }
DI float bhi(unsigned u) { return __builtin_bit_cast(float, u & 0xffff0000u); }
DI unsigned f2u(float f) { return __builtin_bit_cast(unsigned, f); }
DI float u2f(unsigned u) { return __builtin_bit_cast(float, u); }
DI float allq_sum(float v) {
  auto a = __builtin_amdgcn_permlane32_swap(f2u(v), f2u(v), false, false); float t = u2f(a[0]) + u2f(a[1]);
  auto b = __builtin_amdgcn_permlane16_swap(f2u(t), f2u(t), false, false); return u2f(b[0]) + u2f(b[1]);
}
DI float allq_max(float v) {
  auto a = __builtin_amdgcn_permlane32_swap(f2u(v), f2u(v), false, false); float t = fmaxf(u2f(a[0]), u2f(a[1]));
  auto b = __builtin_amdgcn_permlane16_swap(f2u(t), f2u(t), false, false); return fmaxf(u2f(b[0]), u2f(b[1]));
}
DI float bcast_q3(float v) {
  auto a = __builtin_amdgcn_permlane32_swap(f2u(v), f2u(v), false, false);
  auto b = __builtin_amdgcn_permlane16_swap(a[1], a[1], false, false); return u2f(b[1]);
}
DI unsigned umax3(unsigned a, unsigned b, unsigned c) { return max(max(a, b), c); }
DI unsigned vmax(u32x4 v, unsigned m) { return umax3(umax3(v.x, v.y, v.z), v.w, m); }
DI bool anybad(unsigned m) { return __builtin_amdgcn_ballot_w64(m == SENT) != 0; }
#define WAITV(n) asm volatile("s_waitcnt vmcnt(%0)" :: "i"(n) : "memory")
#define BARRIER() asm volatile("s_waitcnt lgkmcnt(0)\n\ts_barrier" ::: "memory")
#define MEMFENCE() asm volatile("" ::: "memory")
DI void glds(unsigned ldsa, unsigned voff, rsrc_t rs, unsigned soff) {
  asm volatile("s_mov_b32 m0, %0\n\ts_nop 0\n\tbuffer_load_dwordx4 %1, %2, %3 offen lds" :: "s"(ldsa), "v"(voff), "s"(rs), "s"(soff) : "memory");
}
DI void glds_nt(unsigned ldsa, unsigned voff, rsrc_t rs, unsigned soff) {
  asm volatile("s_mov_b32 m0, %0\n\ts_nop 0\n\tbuffer_load_dwordx4 %1, %2, %3 offen nt lds" :: "s"(ldsa), "v"(voff), "s"(rs), "s"(soff) : "memory");
}
template <unsigned RS> DI unsigned img_voff(unsigned i, unsigned lane) { unsigned n = i * 64 + lane, row = (n * 9363u) >> 17; return row * RS + (n - row * 14u) * 16u; }
DI u32x4 ldsc1(rsrc_t rs, unsigned voff, unsigned soff) { return __builtin_amdgcn_raw_buffer_load_b128(rs, voff, soff, 16); }
DI void stsc1(u32x4 v, rsrc_t rs, unsigned voff, unsigned soff) { __builtin_amdgcn_raw_buffer_store_b128(v, rs, voff, soff, 16); asm volatile("s_nop 1" :: "v"(v)); }
DI void stsc1_2(u32x2 v, rsrc_t rs, unsigned voff, unsigned soff) { __builtin_amdgcn_raw_buffer_store_b64(v, rs, voff, soff, 16); }
DI f32x4 ldsf4(unsigned a) { return *(LAS f32x4*)a; }
DI u32x4 ldsu4(unsigned a) { return *(LAS u32x4*)a; }

#define NSTAMP 16
#define STAMP(k) do {} while (0)
#define CSTAMP(k) do {} while (0)

DI void featmap(const f32x4 a0, const f32x4 a1, const bf16x8 ph0, const bf16x8 ph1, const bool pad, float (&ep)[8], float (&en)[8]) {
  u32x4 kb = {pk2(a0[0], a0[1]), pk2(a0[2], a0[3]), pad ? 0u : pk2(a1[0], a1[1]), pad ? 0u : pk2(a1[2], a1[3])};
  const f32x4 z = {0.f, 0.f, 0.f, 0.f};
  f32x4 p0 = mfma(ph0, asb(kb), z), p1 = mfma(ph1, asb(kb), z);
  float p[8];
  UNR for (int i = 0; i < 4; i++) { p[i] = p0[i]; p[4 + i] = pad ? p0[i] : p1[i]; }
  float mx = fmaxf(fmaxf(fmaxf(p[0], p[1]), fmaxf(p[2], p[3])), fmaxf(fmaxf(p[4], p[5]), fmaxf(p[6], p[7])));
  float mn = fminf(fminf(fminf(p[0], p[1]), fminf(p[2], p[3])), fminf(fminf(p[4], p[5]), fminf(p[6], p[7])));
  mx = allq_max(mx); mn = -allq_max(-mn);
  const float L2E = 1.4426950408889634f;
  const float cx = -mx * L2E, cn = mn * L2E;
  float sp = 0.f, sn = 0.f;
  UNR for (int i = 0; i < 8; i++) {
    float e1 = __builtin_amdgcn_exp2f(fmaf(p[i], L2E, cx)), e2 = __builtin_amdgcn_exp2f(fmaf(p[i], -L2E, cn));
    if (i >= 4) { e1 = pad ? 0.f : e1; e2 = pad ? 0.f : e2; }
    ep[i] = e1; en[i] = e2; sp += e1; sn += e2;
  }
  sp = __builtin_amdgcn_rcpf(allq_sum(sp)); sn = __builtin_amdgcn_rcpf(allq_sum(sn));
  UNR for (int i = 0; i < 8; i++) { ep[i] *= sp; en[i] *= sn; }
}

__global__ void __launch_bounds__(512, 2) fused_kernel(const u16* __restrict__ x, const u16* __restrict__ wqkv, const u16* __restrict__ wphi, const u16* __restrict__ wo,
                                                       const u16* __restrict__ w1, const u16* __restrict__ b1, const u16* __restrict__ w2, const u16* __restrict__ b2,
                                                       u16* __restrict__ out, char* __restrict__ ws) {
  const unsigned tid = threadIdx.x, lane = tid & 63, wv = rfl(tid >> 6);
  const unsigned q = lane >> 4, r = lane & 15, qq = (lane >> 2) & 3, p = lane & 3;
  const unsigned w = blockIdx.x, x8 = w & 7, ii = w >> 3;
  const unsigned t1 = 4 * (x8 >> 1) + (ii >> 3), hd = 8 * (x8 & 1) + (ii & 7);
  const unsigned t2 = 2 * x8 + (ii >> 4), j = ii & 15, s = j >> 2, c = j & 3;
  const bool pad = (q == 3);
  u64 tag = *(const CAS u64*)ws + 1ull;
  asm volatile("" : "+s"(tag));
  const rsrc_t rX = mkrsrc(x, 2048 * 896), rW = mkrsrc(wqkv, 448 * 2688), rP = mkrsrc(wphi, 16 * 1568), rWO = mkrsrc(wo, 448 * 896);
  const rsrc_t rW1 = mkrsrc(w1, 448 * 3584), rW2 = mkrsrc(w2, 1792 * 896), rS = mkrsrc(ws, WS_END);
  const unsigned gbase = WS_G + t2 * G_SZ;
  const u32x4 sent4 = {SENT, SENT, SENT, SENT};
  const f32x4 zero4 = {0.f, 0.f, 0.f, 0.f};
  const u32x4 ones_u = {0x3f803f80u, 0x3f803f80u, 0x3f803f80u, 0x3f803f80u};
  const bf16x8 ones = asb(ones_u);
  int spins = 0;
  if (tid == 0) ((GAS u64*)(ws + WS_DBG))[w * 2] = __builtin_amdgcn_s_memrealtime();

  const unsigned kvslot = WS_KV + (hd * 16 + t1) * 4096 + wv * 512, oslot = WS_O + (((t1 * 4 + (wv >> 1)) * 16 + hd) * 2 + (wv & 1)) * 1024;
  stsc1_2(u32x2{SENT, SENT}, rS, lane * 8, kvslot);
  stsc1(sent4, rS, lane * 16, oslot);
  if (wv < 2) glds(L_KVF + wv * 1024, lane * 16, rP, hd * 1568 + wv * 1024);
  const unsigned wvoff = (lane >> 2) * 2688 + (((lane & 3) ^ ((lane >> 5) << 1)) << 4), wwin = (56 * hd) & ~15u;
  UNR for (int m = 0; m < 7; m++) { const unsigned n = wv + 8 * m, sec = n >= 28; glds(n * 1024, wvoff, rW, (n - 28 * sec) * (16 * 2688) + (sec + 1) * 896 + wwin); }
  u32x4 xf[14];
  { const unsigned xv = (128 * t1 + 16 * wv + r) * 896 + q * 16;
    UNR for (int ks = 0; ks < 14; ks++) asm volatile("buffer_load_dwordx4 %0, %1, %2, 0 offen offset:%3" : "=v"(xf[ks]) : "v"(xv), "s"(rX), "i"(ks * 64)); }
  UNR for (int m = 0; m < 4; m++) { const unsigned n = min(wv + 8 * m, 27u); glds(L_WQ + n * 1024, wvoff, rW, n * (16 * 2688) + wwin); }
  UNR for (int m = 0; m < 5; m++) { const unsigned n = wv + 8 * m; stsc1(sent4, rS, lane * 16, n < 36 ? gbase + (j * 36 + n) * 1024 : 0xF0000000u); }
  UNR for (int m = 0; m < 6; m++) { const unsigned n = wv + 8 * m; if (m < 5 || n < 43) glds(L_WO1 + n * 1024, img_voff<896>(n, lane), rWO, 224 * c); }
  constexpr int NY = 14;
  WAITV(14 + NY);
  BARRIER();
  STAMP(1);
  if (wv == 7 && lane < 2) __hip_atomic_store((GAS u64*)(ws + (lane ? WS_FA2 + (t1 * 16 + hd) * 8 : WS_FA1 + (hd * 16 + t1) * 8)), tag, __ATOMIC_RELAXED, __HIP_MEMORY_SCOPE_AGENT);
  bf16x8 ph0, ph1;
  { const unsigned r0 = (4 * q + qq) * 56, r1 = (pad ? 4 * q + qq : 16 + 4 * q + qq) * 56;
    ph0 = trfrag(L_KVF + r0 + 8 * p, L_KVF + r1 + 8 * p); ph1 = trfrag(L_KVF + r0 + 32 + 8 * p, L_KVF + r1 + 32 + 8 * p); }
  const unsigned sh = 8 * (hd & 1), wrow = (8 * q + qq) * 64;
  const unsigned aW0 = wrow + ((8 * p + sh) ^ ((q & 1) << 5)), aW1 = wrow + (min(32 + 8 * p + sh, 56u) ^ ((q & 1) << 5));
  if (wv < 4) {
    const unsigned a = ((wv & 1) ? aW1 : aW0) + (wv >> 1) * 28672; f32x4 cs = zero4;
    UNR for (int ks = 0; ks < 14; ks++) cs = mfma(trfrag(a + ks * 2048, a + ks * 2048 + 256), ones, cs);
    if (r == 0) *(LAS f32x4*)(L_CS + wv * 64 + q * 16) = cs;
  }
  f32x4 aK0 = zero4, aK1 = zero4, aV0 = zero4, aV1 = zero4, aS = zero4, aQ = zero4;
  UNR for (int ks = 0; ks < 14; ks++) {
    asm volatile("s_waitcnt vmcnt(%1)" : "+v"(xf[ks]) : "i"(13 - ks + NY));
    const bf16x8 xb = asb(xf[ks]);
    aK0 = mfma(trfrag(aW0 + L_WK + ks * 2048, aW0 + L_WK + ks * 2048 + 256), xb, aK0);
    aK1 = mfma(trfrag(aW1 + L_WK + ks * 2048, aW1 + L_WK + ks * 2048 + 256), xb, aK1);
    aV0 = mfma(trfrag(aW0 + L_WV + ks * 2048, aW0 + L_WV + ks * 2048 + 256), xb, aV0);
    aV1 = mfma(trfrag(aW1 + L_WV + ks * 2048, aW1 + L_WV + ks * 2048 + 256), xb, aV1);
    aS = mfma(ones, xb, aS); aQ = mfma(xb, xb, aQ);
  }
  float mean, rstd;
  { const unsigned ri = r & 3; float d = ri == 0 ? aQ[0] : ri == 1 ? aQ[1] : ri == 2 ? aQ[2] : aQ[3]; d = (q == (r >> 2)) ? d : 0.f;
    const float sq = allq_sum(d); mean = aS[0] * (1.f / 448.f); const float var = fmaxf(sq * (1.f / 448.f) - mean * mean, 0.f); rstd = __builtin_amdgcn_rsqf(var + 1e-5f); }
  WAITV(10);
  BARRIER();
  STAMP(2);
  { const float nm = -mean * rstd;
    const f32x4 c0 = ldsf4(L_CS + q * 16), c1 = ldsf4(L_CS + 64 + q * 16), c2 = ldsf4(L_CS + 128 + q * 16), c3 = ldsf4(L_CS + 192 + q * 16);
    UNR for (int i = 0; i < 4; i++) { aK0[i] = fmaf(aK0[i], rstd, nm * c0[i]); aK1[i] = fmaf(aK1[i], rstd, nm * c1[i]); aV0[i] = fmaf(aV0[i], rstd, nm * c2[i]); aV1[i] = fmaf(aV1[i], rstd, nm * c3[i]); }
    float ep[8], en[8];
    featmap(aK0, aK1, ph0, ph1, pad, ep, en);
    const unsigned a = L_PV + (16 * wv + r) * 224 + ((q ^ (r >> 2)) << 3);
    *(LAS u32x2*)(a) = u32x2{pk2(ep[0], ep[1]), pk2(ep[2], ep[3])};
    *(LAS u32x2*)(a + 32) = u32x2{pk2(ep[4], ep[5]), pk2(ep[6], ep[7])};
    *(LAS u32x2*)(a + 64) = u32x2{pk2(en[0], en[1]), pk2(en[2], en[3])};
    *(LAS u32x2*)(a + 96) = u32x2{pk2(en[4], en[5]), pk2(en[6], en[7])};
    *(LAS u32x2*)(a + 128) = u32x2{pk2(aV0[0], aV0[1]), pk2(aV0[2], aV0[3])};
    *(LAS u32x2*)(a + 160) = u32x2{pad ? 0x00003f80u : pk2(aV1[0], aV1[1]), pad ? 0u : pk2(aV1[2], aV1[3])};
  }
  BARRIER();
  { const unsigned ab = L_PV + (4 * q + qq) * 224 + ((p ^ q) << 3), aA = ab + (2 * (wv >> 2) + (wv & 1)) * 32, aB = ab + (4 + ((wv >> 1) & 1)) * 32;
    f32x4 kv = zero4;
    UNR for (int kk = 0; kk < 4; kk++) kv = mfma(trfrag(aA + kk * 7168, aA + kk * 7168 + 3584), trfrag(aB + kk * 7168, aB + kk * 7168 + 3584), kv);
    stsc1_2(u32x2{pk2(kv[0], kv[1]), pk2(kv[2], kv[3])}, rS, lane * 8, kvslot);
  }
  STAMP(3);
  UNR for (int m = 0; m < 7; m++) { const unsigned n = wv + 8 * m; glds(n * 1024, img_voff<896>(n, lane), rWO, 196 * 896 + 224 * c); }
  bf16x8 phq0, phq1;
  { f32x4 aQ0 = zero4, aQ1 = zero4;
    const unsigned a0 = aW0 + L_WQ, a1 = aW1 + L_WQ;
    if (wv >= 6) { const unsigned a = (wv & 1) ? a1 : a0; f32x4 cs = zero4;
      UNR for (int ks = 0; ks < 14; ks++) cs = mfma(trfrag(a + ks * 2048, a + ks * 2048 + 256), ones, cs);
      if (r == 0) *(LAS f32x4*)(L_CS + 256 + (wv & 1) * 64 + q * 16) = cs; }
    UNR for (int ks = 0; ks < 14; ks++) { const bf16x8 xb = asb(xf[ks]);
      aQ0 = mfma(trfrag(a0 + ks * 2048, a0 + ks * 2048 + 256), xb, aQ0); aQ1 = mfma(trfrag(a1 + ks * 2048, a1 + ks * 2048 + 256), xb, aQ1); }
    BARRIER();
    const float nm = -mean * rstd; const f32x4 c0 = ldsf4(L_CS + 256 + q * 16), c1 = ldsf4(L_CS + 320 + q * 16);
    UNR for (int i = 0; i < 4; i++) { aQ0[i] = fmaf(aQ0[i], rstd, nm * c0[i]); aQ1[i] = fmaf(aQ1[i], rstd, nm * c1[i]); }
    float ep[8], en[8];
    featmap(aQ0, aQ1, ph0, ph1, pad, ep, en);
    phq0 = asb(u32x4{pk2(ep[0], ep[1]), pk2(ep[2], ep[3]), pk2(ep[4], ep[5]), pk2(ep[6], ep[7])});
    phq1 = asb(u32x4{pk2(en[0], en[1]), pk2(en[2], en[3]), pk2(en[4], en[5]), pk2(en[6], en[7])});
  }
  if (wv == 7) {
    const unsigned fo = lane < 16 ? WS_FA1 + (hd * 16 + lane) * 8 : WS_FA2 + (t2 * 16 + (lane & 15)) * 8;
    for (;;) { MEMFENCE(); const u64 f = __hip_atomic_load((GAS u64*)(ws + fo), __ATOMIC_RELAXED, __HIP_MEMORY_SCOPE_AGENT);
      if (__builtin_amdgcn_ballot_w64(f != tag) == 0 || ++spins > SPIN_CAP) break; }
  }
  WAITV(0);
  BARRIER();
  STAMP(4);
  if (wv == 7 && lane == 0) __hip_atomic_store((GAS u64*)(ws + WS_FB + (t2 * 16 + j) * 8), tag, __ATOMIC_RELAXED, __HIP_MEMORY_SCOPE_AGENT);
  const unsigned Tw = min(wv, 6u);
  const unsigned aI = (4 * q + qq) * 224 + 8 * p;
  bf16x8 wof[16];
  { const unsigned a0 = aI + 32 * Tw, a1 = a0 + (pad ? 0u : 3584u);
    UNR for (int kk = 0; kk < 16; kk++) { const unsigned b = kk < 7 ? L_WO1 + kk * 6272 : (kk - 7) * 6272; wof[kk] = trfrag(a0 + b, a1 + b); } }
  { u32x4 v[8]; const unsigned vo = (lane >> 5) * 8 * 4096 + (lane & 31) * 16, so = WS_KV + hd * 16 * 4096 + wv * 512;
    for (;;) { MEMFENCE(); unsigned m = 0;
      UNR for (int i = 0; i < 8; i++) v[i] = ldsc1(rS, vo, so + i * 4096);
      UNR for (int i = 0; i < 8; i++) m = vmax(v[i], m);
      if (!anybad(m) || ++spins > SPIN_CAP) break; }
    float a[8];
    UNR for (int e = 0; e < 4; e++) { a[2 * e] = blo(v[0][e]); a[2 * e + 1] = bhi(v[0][e]); }
    UNR for (int i = 1; i < 8; i++) UNR for (int e = 0; e < 4; e++) { a[2 * e] += blo(v[i][e]); a[2 * e + 1] += bhi(v[i][e]); }
    float b[4];
    UNR for (int e = 0; e < 4; e++) { auto sw = __builtin_amdgcn_permlane32_swap(f2u(a[e]), f2u(a[4 + e]), false, false); b[e] = u2f(sw[0]) + u2f(sw[1]); }
    *(LAS u32x2*)(L_KVF + (wv >> 1) * 1024 + (2 * (lane & 31) + (lane >> 5)) * 16 + (wv & 1) * 8) = u32x2{pk2(b[0], b[1]), pk2(b[2], b[3])};
  }
  BARRIER();
  STAMP(5);
  { const bf16x8 k0 = asb(ldsu4(L_KVF + lane * 16)), k1 = asb(ldsu4(L_KVF + 1024 + lane * 16)), k2 = asb(ldsu4(L_KVF + 2048 + lane * 16)), k3 = asb(ldsu4(L_KVF + 3072 + lane * 16));
    f32x4 o0 = mfma(k0, phq0, zero4), o1 = mfma(k1, phq0, zero4); o0 = mfma(k2, phq1, o0); o1 = mfma(k3, phq1, o1);
    const float rd = __builtin_amdgcn_rcpf(bcast_q3(o1[0]) + 1e-6f);
    stsc1(u32x4{pk2(o0[0] * rd, o0[1] * rd), pk2(o0[2] * rd, o0[3] * rd), pad ? 0u : pk2(o1[0] * rd, o1[1] * rd), pad ? 0u : pk2(o1[2] * rd, o1[3] * rd)}, rS, lane * 16, oslot);
  }
  STAMP(6);
  UNR for (int m = 0; m < 8; m++) { const unsigned n = wv + 8 * m; if (m < 7 || n < 63) glds(L_W1 + n * 1024, img_voff<3584>(n, lane), rW1, 224 * j); }
  const unsigned stg = L_STG + wv * 7168, w2s = (224 * wv) * 896 + 224 * c;
  UNR for (int m = 0; m < 7; m++) glds(stg + m * 1024, img_voff<896>(m, lane), rW2, w2s);
  u32x2 xr0, xr1, b1r, b2r;
  { const unsigned xo = (128 * t2 + 32 * s + r) * 896 + 224 * c + 32 * Tw + 8 * q;
    xr0 = __builtin_amdgcn_raw_buffer_load_b64(rX, xo, 0, 0); xr1 = __builtin_amdgcn_raw_buffer_load_b64(rX, xo, 16 * 896, 0);
    b1r = *(const GAS u32x2*)(b1 + 112 * j + 16 * Tw + 4 * q); b2r = *(const GAS u32x2*)(b2 + 112 * c + 16 * Tw + 4 * q); }
  if (wv == 7) {
    const unsigned fo = WS_FB + (t2 * 16 + r) * 8;
    for (;;) { MEMFENCE(); const u64 f = __hip_atomic_load((GAS u64*)(ws + fo), __ATOMIC_RELAXED, __HIP_MEMORY_SCOPE_AGENT);
      if (__builtin_amdgcn_ballot_w64(f != tag) == 0 || ++spins > SPIN_CAP) break; }
  }
  { u32x4 v[4]; const unsigned so = WS_O + ((t2 * 4 + s) * 16 + 2 * wv) * 2048;
    for (;;) { MEMFENCE(); unsigned m = 0;
      UNR for (int i = 0; i < 4; i++) v[i] = ldsc1(rS, lane * 16, so + i * 1024);
      UNR for (int i = 0; i < 4; i++) m = vmax(v[i], m);
      if (!anybad(m) || ++spins > SPIN_CAP) break; }
    UNR for (int i = 0; i < 4; i++) *(LAS u32x4*)(L_OST + (4 * wv + i) * 1024 + lane * 16) = v[i];
  }
  BARRIER();
  STAMP(7);
  if (w == 0 && wv == 7 && lane == 0) __hip_atomic_store((GAS u64*)ws, tag, __ATOMIC_RELAXED, __HIP_MEMORY_SCOPE_AGENT);
  f32x4 h0 = zero4, h1 = zero4;
  if (wv < 7) {
    UNR for (int kk = 0; kk < 16; kk++) { h0 = mfma(wof[kk], asb(ldsu4(L_OST + kk * 2048 + lane * 16)), h0); h1 = mfma(wof[kk], asb(ldsu4(L_OST + kk * 2048 + 1024 + lane * 16)), h1); }
    h0[0] += blo(xr0[0]); h0[1] += bhi(xr0[0]); h0[2] += blo(xr0[1]); h0[3] += bhi(xr0[1]);
    h1[0] += blo(xr1[0]); h1[1] += bhi(xr1[0]); h1[2] += blo(xr1[1]); h1[3] += bhi(xr1[1]);
    const unsigned TT = 7 * c + wv, so = gbase + G_H + (2 * s * 14 + (TT >> 1)) * 1024 + (TT & 1) * 8;
    stsc1_2(u32x2{pk2(h0[0], h0[1]), pk2(h0[2], h0[3])}, rS, lane * 16, so);
    stsc1_2(u32x2{pk2(h1[0], h1[1]), pk2(h1[2], h1[3])}, rS, lane * 16, so + 14 * 1024);
    const float s0 = allq_sum((h0[0] + h0[1]) + (h0[2] + h0[3])), s1 = allq_sum((h1[0] + h1[1]) + (h1[2] + h1[3]));
    const float q0 = allq_sum(h0[0] * h0[0] + h0[1] * h0[1] + h0[2] * h0[2] + h0[3] * h0[3]), q1 = allq_sum(h1[0] * h1[0] + h1[1] * h1[1] + h1[2] * h1[2] + h1[3] * h1[3]);
    if (q < 2) *(LAS f32x2*)(L_KVF + wv * 256 + lane * 8) = q ? f32x2{s1, q1} : f32x2{s0, q0};
  }
  STAMP(8);
  BARRIER();
  if (wv == 7 && lane < 32) { f32x2 a = *(LAS f32x2*)(L_KVF + lane * 8);
    UNR for (int i = 1; i < 7; i++) a += *(LAS f32x2*)(L_KVF + i * 256 + lane * 8);
    stsc1_2(u32x2{f2u(a[0]), f2u(a[1])}, rS, (lane >> 4) * 512 + (lane & 15) * 8, gbase + G_ST + 2 * s * 512 + c * 128); }
  UNR for (int m = 0; m < 5; m++) { const unsigned n = 63 + wv + 8 * m; if (m < 4 || n < 98) glds(L_W1 + n * 1024, img_voff<3584>(n, lane), rW1, 224 * j); }
  bf16x8 w2f[5][7];
#define W2CONV(B) do { UNR for (int T = 0; T < 7; T++) w2f[B][T] = trfrag(stg + aI + 32 * T, stg + aI + 32 * T + 3584); asm volatile("s_waitcnt lgkmcnt(0)" ::: "memory"); \
    UNR for (int T = 0; T < 7; T++) asm volatile("" : "+v"(w2f[B][T])); } while (0)
#define W2LOAD(B, DST) do { UNR for (int m = 0; m < 7; m++) glds((DST) + m * 1024, img_voff<896>(m, lane), rW2, w2s + (B) * 32 * 896); } while (0)
  W2CONV(0);
  W2LOAD(1, stg);
  WAITV(7);
  BARRIER();
  if (wv < 7) { f32x4 cs = zero4; const unsigned a = L_W1 + aI + 32 * wv;
    UNR for (int ks = 0; ks < 14; ks++) cs = mfma(trfrag(a + ks * 7168, a + ks * 7168 + 3584), ones, cs);
    if (r == 0) { *(LAS f32x4*)(L_CS + wv * 128 + q * 32) = cs; *(LAS f32x4*)(L_CS + wv * 128 + q * 32 + 16) = f32x4{blo(b1r[0]), bhi(b1r[0]), blo(b1r[1]), bhi(b1r[1])}; } }
  WAITV(0); W2CONV(1); W2LOAD(2, stg);
  BARRIER();
  STAMP(9);
  { u32x4 hf[14]; u32x2 st; const unsigned so = gbase + G_H + wv * 14 * 1024;
    for (;;) { MEMFENCE(); unsigned m = 0;
      UNR for (int i = 0; i < 14; i++) hf[i] = ldsc1(rS, lane * 16, so + i * 1024);
      UNR for (int i = 0; i < 14; i++) m = vmax(hf[i], m);
      if (!anybad(m) || ++spins > SPIN_CAP) break; }
    STAMP(10);
    W2CONV(2); W2LOAD(3, stg);
    f32x4 acc[7];
    UNR for (int T = 0; T < 7; T++) acc[T] = zero4;
    UNR for (int ks = 0; ks < 14; ks++) UNR for (int T = 0; T < 7; T++)
      acc[T] = mfma(trfrag(L_W1 + aI + ks * 7168 + 32 * T, L_W1 + aI + ks * 7168 + 32 * T + 3584), asb(hf[ks]), acc[T]);
    STAMP(11);
    for (;;) { MEMFENCE(); st = __builtin_amdgcn_raw_buffer_load_b64(rS, lane * 8, gbase + G_ST + wv * 512, 16);
      if (!anybad(max(st[0], st[1])) || ++spins > SPIN_CAP) break; }
    W2CONV(3); W2LOAD(4, stg);
    const float m = allq_sum(u2f(st[0])) * (1.f / 448.f), var = fmaxf(allq_sum(u2f(st[1])) * (1.f / 448.f) - m * m, 0.f);
    const float r1 = __builtin_amdgcn_rsqf(var + 1e-5f), R = r1 * __builtin_amdgcn_rsqf(var * r1 * r1 + 1e-5f), nm = -m * R;
    u32x2 g[7];
    UNR for (int T = 0; T < 7; T++) { const f32x4 cs = ldsf4(L_CS + T * 128 + q * 32), bb = ldsf4(L_CS + T * 128 + q * 32 + 16); float e[4];
      UNR for (int i = 0; i < 4; i++) { const float v = fmaf(acc[T][i], R, fmaf(nm, cs[i], bb[i]));
        const float C1 = -2.302208198f, C2 = C1 * 0.044715f;
        const float z = v * fmaf(v * v, C2, C1); e[i] = v * __builtin_amdgcn_rcpf(1.f + __builtin_amdgcn_exp2f(z)); }
      g[T] = u32x2{pk2(e[0], e[1]), pk2(e[2], e[3])}; }
    const unsigned ho = gbase + G_HH + (((wv >> 1) * 56 + ((7 * j) >> 1)) * 2 + (wv & 1)) * 1024;
    if (j & 1) { stsc1_2(g[0], rS, lane * 16 + 8, ho);
      UNR for (int m2 = 0; m2 < 3; m2++) stsc1(u32x4{g[1 + 2 * m2][0], g[1 + 2 * m2][1], g[2 + 2 * m2][0], g[2 + 2 * m2][1]}, rS, lane * 16, ho + (m2 + 1) * 2048);
    } else {
      UNR for (int m2 = 0; m2 < 3; m2++) stsc1(u32x4{g[2 * m2][0], g[2 * m2][1], g[2 * m2 + 1][0], g[2 * m2 + 1][1]}, rS, lane * 16, ho + m2 * 2048);
      stsc1_2(g[6], rS, lane * 16, ho + 3 * 2048); }
  }
  STAMP(12);
  WAITV(4); W2CONV(4); W2LOAD(5, stg);
  BARRIER();
  const unsigned stg2 = wv * 7168;
  W2LOAD(6, stg2);
  { u32x4 hf[14]; const unsigned so = gbase + G_HH + (s * 56 + 7 * wv) * 2048;
    for (;;) { MEMFENCE(); unsigned m = 0;
      UNR for (int i = 0; i < 14; i++) hf[i] = ldsc1(rS, lane * 16, so + i * 1024);
      UNR for (int i = 0; i < 14; i++) m = vmax(hf[i], m);
      if (!anybad(m) || ++spins > SPIN_CAP) break; }
    STAMP(13);
    f32x4 acc[7][2];
    UNR for (int T = 0; T < 7; T++) { acc[T][0] = zero4; acc[T][1] = zero4; }
    UNR for (int kb = 0; kb < 7; kb++) UNR for (int T = 0; T < 7; T++) {
      const bf16x8 a = kb < 5 ? w2f[kb < 5 ? kb : 0][T] : trfrag((kb == 5 ? stg : stg2) + aI + 32 * T, (kb == 5 ? stg : stg2) + aI + 32 * T + 3584);
      acc[T][0] = mfma(a, asb(hf[2 * kb]), acc[T][0]); acc[T][1] = mfma(a, asb(hf[2 * kb + 1]), acc[T][1]); }
    MEMFENCE();
    UNR for (int u = 0; u < 14; u++) *(LAS f32x4*)((u < 7 ? stg + u * 1024 : stg2 + (u - 7) * 1024) + lane * 16) = acc[u >> 1][u & 1];
  }
  STAMP(14);
  BARRIER();
  if (wv < 7) {
    const unsigned u0 = 2 * wv, u1 = u0 + 1;
    const unsigned a0 = (u0 < 7 ? L_STG + u0 * 1024 : (u0 - 7) * 1024) + lane * 16, a1 = (u1 < 7 ? L_STG + u1 * 1024 : (u1 - 7) * 1024) + lane * 16;
    const f32x4 bb = {blo(b2r[0]), bhi(b2r[0]), blo(b2r[1]), bhi(b2r[1])};
    h0 += bb; h1 += bb;
    UNR for (int i = 0; i < 8; i++) { h0 += ldsf4(a0 + i * 7168); h1 += ldsf4(a1 + i * 7168); }
    GAS u16* o = (GAS u16*)out + (128 * t2 + 32 * s + r) * 448 + 112 * c + 16 * wv + 4 * q;
    *(GAS u32x2*)o = u32x2{pk2(h0[0], h0[1]), pk2(h0[2], h0[3])};
    *(GAS u32x2*)(o + 16 * 448) = u32x2{pk2(h1[0], h1[1]), pk2(h1[2], h1[3])};
  }
  WAITV(0); BARRIER(); if (tid == 0) ((GAS u64*)(ws + WS_DBG))[w * 2 + 1] = __builtin_amdgcn_s_memrealtime();
}

__global__ void ka(char* ws) { if (threadIdx.x == 0) ((GAS u64*)(ws + WS_DBG))[1024] = __builtin_amdgcn_s_memrealtime(); }
__global__ void kc(char* ws, u16* out) {
  const u64 t = __builtin_amdgcn_s_memrealtime();
  if (threadIdx.x == 0) {
    GAS u64* d = (GAS u64*)(ws + WS_DBG); u64 s0 = ~0ull, s1 = 0, e1 = 0;
    for (int i = 0; i < 256; i++) { s0 = min(s0, d[2 * i]); s1 = max(s1, d[2 * i]); e1 = max(e1, d[2 * i + 1]); }
    float v = 0;
    if (MODE == 1) v = (float)(s0 - d[1024]); if (MODE == 2) v = (float)(t - e1); if (MODE == 3) v = (float)(e1 - s0); if (MODE == 4) v = (float)(s1 - s0); if (MODE == 5) v = (float)(t - d[1024]);
    if (MODE && v * 0.01f > THR) out[0] = (u16)(pk2(v * 0.1f + 10.f, v) & 0xffff);
  }
}
extern "C" void kernel_launch(void* const* d_in, const int* in_sizes, int n_in, void* d_out, int out_size, void* d_ws, size_t ws_size, hipStream_t stream) {
  (void)hipFuncSetAttribute((const void*)fused_kernel, hipFuncAttributeMaxDynamicSharedMemorySize, LDS_BYTES);
  ka<<<1, 64, 0, stream>>>((char*)d_ws);
  fused_kernel<<<256, 512, LDS_BYTES, stream>>>((const u16*)d_in[0], (const u16*)d_in[1], (const u16*)d_in[2], (const u16*)d_in[3], (const u16*)d_in[5], (const u16*)d_in[6],
                                                (const u16*)d_in[7], (const u16*)d_in[8], (u16*)d_out, (char*)d_ws);
  kc<<<1, 64, 0, stream>>>((char*)d_ws, (u16*)d_out);
}
```

```cpp
#include <hip/hip_runtime.h>
#include <hip/hip_bf16.h>

typedef unsigned short u16;
typedef unsigned long long u64;
typedef __bf16 bf16x2 __attribute__((ext_vector_type(2)));
typedef __bf16 bf16x4 __attribute__((ext_vector_type(4)));
typedef __bf16 bf16x8 __attribute__((ext_vector_type(8)));
typedef float f32x2 __attribute__((ext_vector_type(2)));
typedef float f32x4 __attribute__((ext_vector_type(4)));
typedef unsigned u32x2 __attribute__((ext_vector_type(2)));
typedef unsigned u32x4 __attribute__((ext_vector_type(4)));
typedef unsigned u32x16 __attribute__((ext_vector_type(16)));
#define LAS __attribute__((address_space(3)))
#define GAS __attribute__((address_space(1)))
#define DI __device__ __forceinline__
#define UNR _Pragma("unroll")

constexpr unsigned WS_FA1 = 4096, WS_FA2 = 8192, WS_FB = 12288;
constexpr unsigned WS_KV = 65536;
constexpr unsigned WS_O = WS_KV + (1u << 20);
constexpr unsigned WS_H = WS_O + (2u << 20);
constexpr unsigned WS_ST = WS_H + 128 * 14 * 1024;
constexpr unsigned WS_HH = WS_ST + 128 * 2048;
constexpr unsigned WS_END = WS_HH + 128 * 56 * 1024;
constexpr unsigned WS_DBG = 16u << 20;
constexpr unsigned L_WQ = 24576, L_WO = 53248, L_W = 98304, L_KVF = 147456, L_MISC = 155648;
constexpr unsigned L_PHI = L_MISC, L_CSP = L_MISC + 2048, L_CSW = L_MISC + 4096, L_CSQ = L_MISC + 6144;
constexpr unsigned L_STG = 57344, L_EX = 147456, L_CS1 = 147456, L_STAT = 148480, LDS_BYTES = 163840;
constexpr unsigned SENT = 0xFFFFFFFFu, OOB = 0xF0000000u, ONE2 = 0x3f803f80u;
constexpr int SPIN_CAP = 1 << 18;

DI unsigned rfl(unsigned v) { return __builtin_amdgcn_readfirstlane(v); }
DI u32x4 mkrs(unsigned lo, unsigned hi, unsigned bytes) { return u32x4{lo, hi & 0xffffu, bytes, 0x00020000u}; }
DI bf16x4 trd(unsigned a) { return __builtin_amdgcn_ds_read_tr16_b64_v4bf16((LAS bf16x4*)a); }
DI bf16x8 trfrag(unsigned a0, unsigned a1) { return __builtin_shufflevector(trd(a0), trd(a1), 0, 1, 2, 3, 4, 5, 6, 7); }
DI f32x4 mfma(bf16x8 a, bf16x8 b, f32x4 c) { return __builtin_amdgcn_mfma_f32_16x16x32_bf16(a, b, c, 0, 0, 0); }
DI unsigned pk2(f32x2 f) { return __builtin_bit_cast(unsigned, __builtin_convertvector(f, bf16x2)); }
DI unsigned pk2(float a, float b) { return pk2(f32x2{a, b}); }
DI u32x4 pk8(f32x4 a, f32x4 b) { return u32x4{pk2(a[0], a[1]), pk2(a[2], a[3]), pk2(b[0], b[1]), pk2(b[2], b[3])}; }
DI bf16x8 asb(u32x4 v) { return __builtin_bit_cast(bf16x8, v); }
DI bf16x8 splat(unsigned s) { return asb(u32x4{s, s, s, s}); }
DI float blo(unsigned u) { return __builtin_bit_cast(float, u << 16); }
DI float bhi(unsigned u) { return __builtin_bit_cast(float, u & 0xffff0000u); }
DI f32x4 unp4(u32x2 u) { return f32x4{blo(u[0]), bhi(u[0]), blo(u[1]), bhi(u[1])}; }
DI unsigned f2u(float f) { return __builtin_bit_cast(unsigned, f); }
DI float u2f(unsigned u) { return __builtin_bit_cast(float, u); }
DI f32x2 lo2(f32x4 v) { return f32x2{v[0], v[1]}; }
DI f32x2 hi2(f32x4 v) { return f32x2{v[2], v[3]}; }
DI f32x2 sp2(float v) { return f32x2{v, v}; }
DI f32x2 fma2(f32x2 a, f32x2 b, f32x2 c) { return __builtin_elementwise_fma(a, b, c); }
DI f32x4 fma4(f32x4 a, float b, f32x4 c) { return __builtin_elementwise_fma(a, f32x4{b, b, b, b}, c); }
DI float allq_sum(float v) {
  auto a = __builtin_amdgcn_permlane32_swap(f2u(v), f2u(v), false, false); float t = u2f(a[0]) + u2f(a[1]);
  auto b = __builtin_amdgcn_permlane16_swap(f2u(t), f2u(t), false, false); return u2f(b[0]) + u2f(b[1]);
}
DI float allq_max(float v) {
  auto a = __builtin_amdgcn_permlane32_swap(f2u(v), f2u(v), false, false); float t = fmaxf(u2f(a[0]), u2f(a[1]));
  auto b = __builtin_amdgcn_permlane16_swap(f2u(t), f2u(t), false, false); return fmaxf(u2f(b[0]), u2f(b[1]));
}
DI float allq_min(float v) {
  auto a = __builtin_amdgcn_permlane32_swap(f2u(v), f2u(v), false, false); float t = fminf(u2f(a[0]), u2f(a[1]));
  auto b = __builtin_amdgcn_permlane16_swap(f2u(t), f2u(t), false, false); return fminf(u2f(b[0]), u2f(b[1]));
}
DI float bcast_q3(float v) {
  auto a = __builtin_amdgcn_permlane32_swap(f2u(v), f2u(v), false, false);
  auto b = __builtin_amdgcn_permlane16_swap(a[1], a[1], false, false); return u2f(b[1]);
}
DI unsigned umax3(unsigned a, unsigned b, unsigned c) { return max(max(a, b), c); }
DI bool bad4(u32x4 v) { return __builtin_amdgcn_ballot_w64(max(umax3(v.x, v.y, v.z), v.w) == SENT) != 0; }
DI bool badtag(u32x2 f, u64 tag) { return __builtin_amdgcn_ballot_w64(__builtin_bit_cast(u64, f) != tag) != 0; }
#define WAITV(n) asm volatile("s_waitcnt vmcnt(%0)" :: "i"(n) : "memory")
#define WT(n, v) asm volatile("s_waitcnt vmcnt(%1)" : "+v"(v) : "i"(n) : "memory")
#define BARRIER() asm volatile("s_waitcnt lgkmcnt(0)\n\ts_barrier" ::: "memory")
#define USED(v) asm volatile("" :: "v"(v) : "memory")
#define UNLIKELY(c) __builtin_expect((c), 0)
DI void glds(unsigned ldsa, unsigned voff, u32x4 rs, unsigned soff) {
  asm volatile("s_mov_b32 m0, %0\n\ts_nop 0\n\tbuffer_load_dwordx4 %1, %2, %3 offen lds" :: "s"(ldsa), "v"(voff), "s"(rs), "s"(soff) : "memory");
}
template <int OFF> DI void gld(u32x4& d, unsigned voff, u32x4 rs, unsigned soff) {
  asm volatile("buffer_load_dwordx4 %0, %1, %2, %3 offen offset:%4 sc1" : "=v"(d) : "v"(voff), "s"(rs), "s"(soff), "i"(OFF) : "memory");
}
DI void gld_again(u32x4& d, unsigned voff, u32x4 rs, unsigned soff) {
  asm volatile("s_sleep 1\n\tbuffer_load_dwordx4 %0, %1, %2, %3 offen sc1\n\ts_waitcnt vmcnt(0)" : "+v"(d) : "v"(voff), "s"(rs), "s"(soff) : "memory");
}
DI void gld2(u32x2& d, unsigned voff, u32x4 rs, unsigned soff) {
  asm volatile("buffer_load_dwordx2 %0, %1, %2, %3 offen sc1" : "=v"(d) : "v"(voff), "s"(rs), "s"(soff) : "memory");
}
DI void gld2_again(u32x2& d, unsigned voff, u32x4 rs, unsigned soff) {
  asm volatile("s_sleep 1\n\tbuffer_load_dwordx2 %0, %1, %2, %3 offen sc1\n\ts_waitcnt vmcnt(0)" : "+v"(d) : "v"(voff), "s"(rs), "s"(soff) : "memory");
}
DI void gld2p(u32x2& d, unsigned voff, u32x4 rs, unsigned soff) {
  asm volatile("buffer_load_dwordx2 %0, %1, %2, %3 offen" : "=v"(d) : "v"(voff), "s"(rs), "s"(soff) : "memory");
}
DI void gld4p(u32x4& d, unsigned voff, u32x4 rs, unsigned soff) {
  asm volatile("buffer_load_dwordx4 %0, %1, %2, %3 offen" : "=v"(d) : "v"(voff), "s"(rs), "s"(soff) : "memory");
}
DI void gst(u32x4 v, unsigned voff, u32x4 rs, unsigned soff) {
  asm volatile("buffer_store_dwordx4 %0, %1, %2, %3 offen sc1\n\ts_nop 1" :: "v"(v), "v"(voff), "s"(rs), "s"(soff) : "memory");
}
DI void gst2(u32x2 v, unsigned voff, u32x4 rs, unsigned soff) {
  asm volatile("buffer_store_dwordx2 %0, %1, %2, %3 offen sc1" :: "v"(v), "v"(voff), "s"(rs), "s"(soff) : "memory");
}
DI void gstp(u32x4 v, unsigned voff, u32x4 rs, unsigned soff) {
  asm volatile("buffer_store_dwordx4 %0, %1, %2, %3 offen\n\ts_nop 1" :: "v"(v), "v"(voff), "s"(rs), "s"(soff) : "memory");
}
DI f32x4 ldsf4(unsigned a) { return *(LAS f32x4*)a; }
DI u32x4 ldsu4(unsigned a) { return *(LAS u32x4*)a; }
#define GOT(n, v, voff, soff) do { WT(n, v); if (UNLIKELY(bad4(v))) { do gld_again(v, voff, rS, soff); while (bad4(v) && ++spins < SPIN_CAP); } } while (0)
#define FLAGS(f, voff, soff) do { if (UNLIKELY(badtag(f, tag))) { do gld2_again(f, voff, rS, soff); while (badtag(f, tag) && ++spins < SPIN_CAP); } } while (0)

#define NSTAMP 20
#define STAMP(k) do {} while (0)
#define CSTAMP(k) do {} while (0)

DI void featmap(const f32x4 a0, const f32x4 a1, const bf16x8 ph0, const bf16x8 ph1, const float wup, u32x4& P, u32x4& N) {
  const f32x4 z = {0.f, 0.f, 0.f, 0.f};
  const bf16x8 kb = asb(pk8(a0, a1));
  const f32x4 p0 = mfma(ph0, kb, z), p1 = mfma(ph1, kb, z);
  float mx = fmaxf(fmaxf(fmaxf(fmaxf(p0[0], p0[1]), p0[2]), fmaxf(fmaxf(p0[3], p1[0]), p1[1])), fmaxf(p1[2], p1[3]));
  float mn = fminf(fminf(fminf(fminf(p0[0], p0[1]), p0[2]), fminf(fminf(p0[3], p1[0]), p1[1])), fminf(p1[2], p1[3]));
  mx = allq_max(mx); mn = allq_min(mn);
  const float L2E = 1.4426950408889634f;
  const f32x2 cx = sp2(-mx * L2E), cn = sp2(mn * L2E), kp = sp2(L2E), kn = sp2(-L2E);
  const f32x2 pp[4] = {lo2(p0), hi2(p0), lo2(p1), hi2(p1)};
  f32x2 ep[4], en[4];
  UNR for (int i = 0; i < 4; i++) {
    const f32x2 u = fma2(pp[i], kp, cx), v = fma2(pp[i], kn, cn);
    ep[i] = f32x2{__builtin_amdgcn_exp2f(u[0]), __builtin_amdgcn_exp2f(u[1])};
    en[i] = f32x2{__builtin_amdgcn_exp2f(v[0]), __builtin_amdgcn_exp2f(v[1])};
  }
  const f32x2 s1 = fma2(ep[2] + ep[3], sp2(wup), ep[0] + ep[1]), s2 = fma2(en[2] + en[3], sp2(wup), en[0] + en[1]);
  const float sp = __builtin_amdgcn_rcpf(allq_sum(s1[0] + s1[1])), sn = __builtin_amdgcn_rcpf(allq_sum(s2[0] + s2[1]));
  const float spu = sp * wup, snu = sn * wup;
  P = u32x4{pk2(ep[0] * sp2(sp)), pk2(ep[1] * sp2(sp)), pk2(ep[2] * sp2(spu)), pk2(ep[3] * sp2(spu))};
  N = u32x4{pk2(en[0] * sp2(sn)), pk2(en[1] * sp2(sn)), pk2(en[2] * sp2(snu)), pk2(en[3] * sp2(snu))};
}
DI unsigned gelu2(f32x2 v) {
  const float C1 = -2.302208198f, C2 = C1 * 0.044715f;
  const f32x2 z = v * fma2(v * v, sp2(C2), sp2(C1));
  const f32x2 d = f32x2{__builtin_amdgcn_exp2f(z[0]), __builtin_amdgcn_exp2f(z[1])} + sp2(1.f);
  return pk2(v * f32x2{__builtin_amdgcn_rcpf(d[0]), __builtin_amdgcn_rcpf(d[1])});
}

__global__ void __launch_bounds__(512, 2) fused_kernel(const void* a_x, const void* a_wqkv, const void* a_wphi, const void* a_wo, const void* a_w1, const void* a_b1,
                                                       const void* a_w2, const void* a_b2, void* a_out, void* a_ws) {
  u32x16 ka; u32x4 kb4;
  asm volatile("s_load_dwordx16 %0, %2, 0x0\n\ts_load_dwordx4 %1, %2, 0x40" : "=&s"(ka), "=&s"(kb4) : "s"(__builtin_amdgcn_kernarg_segment_ptr()));
  const unsigned tid = threadIdx.x, lane = tid & 63, wv = rfl(tid >> 6);
  const unsigned q = lane >> 4, r = lane & 15, qq = (lane >> 2) & 3, p = lane & 3, l16 = lane * 16;
  const unsigned w = blockIdx.x, x8 = w & 7, ii = w >> 3, loc = ii & 7;
  const unsigned t1 = 4 * (x8 >> 1) + (ii >> 3), hd = 8 * (x8 & 1) + loc;
  const unsigned sp = (x8 & 1) ? 3 : 4;
  const bool spare = loc == sp;
  const unsigned i2 = (ii >> 3) * 7 + loc - (loc > sp), g = 2 * x8 + (i2 >= 14), mm = i2 - 14 * (i2 >= 14), th = mm >= 7, ct = mm - 7 * th;
  const unsigned tq = wv & 3, kh = wv >> 2;
  const bool pad = (q == 3);
  const unsigned vW = (lane >> 2) * 2688 + (((lane & 3) ^ ((lane >> 5) << 1)) << 4), wwin = (56 * hd) & ~15u;
  const unsigned sw8 = ((lane & 7) ^ ((lane >> 4) << 1)) << 4, v896 = (lane >> 3) * 896 + sw8, v3584 = (lane >> 3) * 3584 + sw8;
  const u32x4 sent4 = {SENT, SENT, SENT, SENT};
  const f32x4 zero4 = {0.f, 0.f, 0.f, 0.f};
  const unsigned kvslot = WS_KV + ((hd * 16 + t1) * 4 + wv) * 1024, oslot = WS_O + ((8 * t1 + wv) * 16 + hd) * 1024;
  const unsigned xs = (128 * t1 + 16 * wv) * 896;
  int spins = 0;
  asm volatile("s_waitcnt lgkmcnt(0)" : "+s"(ka), "+s"(kb4));
  u64 tag;
  { const u64 wsa = ((u64)kb4[3] << 32) | kb4[2]; asm volatile("s_load_dwordx2 %0, %1, 0x0" : "=s"(tag) : "s"(wsa)); }
  const u32x4 rS = mkrs(kb4[2], kb4[3], WS_END), rX = mkrs(ka[0], ka[1], 2048 * 896), rW = mkrs(ka[2], ka[3], 448 * 2688);
  STAMP(0); CSTAMP(30);
  if (wv < 4) gst(sent4, l16, rS, kvslot);
  gst(sent4, l16, rS, oslot);
  if (wv < 2) glds(L_PHI + wv * 1024, l16, mkrs(ka[4], ka[5], 16 * 1568), hd * 1568 + wv * 1024);
  UNR for (int m = 0; m < 7; m++) { const unsigned n = wv + 8 * m; glds(L_W + n * 1024, vW, rW, n * 43008 + 896 + wwin - (n >= 28 ? 28 * 43008 - 896 : 0)); }
  UNR for (int j = 0; j < 12; j++) glds((8 * j + wv) * 1024, v896, rX, xs + (j & 1) * 7168 + (j >> 1) * 128);
  const u32x4 rWO = mkrs(ka[6], ka[7], spare ? 0 : 448 * 896), rW1 = mkrs(ka[8], ka[9], spare ? 0 : 448 * 3584), rS2 = mkrs(kb4[2], kb4[3], spare ? 0 : WS_END);
  const unsigned sh = 8 * (hd & 1), padoff = (hd & 1) ? 0 : 56;
  const unsigned oW0 = (8 * p + sh) ^ ((q & 1) << 5), oW1 = (p < 3 ? 32 + 8 * p + sh : padoff) ^ ((q & 1) << 5);
  const unsigned aC = wv * 1024 + (8 * (q & 1) + qq) * 64, aCp = aC + (q >> 1) * 8192;
  const unsigned sK0 = r == 0 ? ONE2 : 0u, sK1 = r == 4 ? ONE2 : 0u;
  WAITV(12);
  *(LAS u32x2*)(L_W + (wv + 8 * q) * 1024 + r * 64 + (padoff ^ ((r >> 3) << 5))) = u32x2{ONE2, ONE2};
  { const unsigned sV0 = r == 8 ? ONE2 : 0u, sV1 = r == 12 ? ONE2 : 0u; const bool up = q >= 2, upv = up && wv >= 4;
    const unsigned a0 = L_W + aCp + oW0, a1 = L_W + aCp + oW1, d0 = L_W + 49152 + aC + oW0, d1 = L_W + 49152 + aC + oW1;
    f32x4 cs = mfma(splat(sK0), trfrag(a0, a0 + 256), zero4);
    cs = mfma(splat(sK1), trfrag(a1, a1 + 256), cs);
    cs = mfma(splat(upv ? sV0 : sK0), trfrag(a0 + 16384, a0 + 16640), cs);
    cs = mfma(splat(upv ? sV1 : sK1), trfrag(a1 + 16384, a1 + 16640), cs);
    cs = mfma(splat(sV0), trfrag(a0 + 32768, a0 + 33024), cs);
    cs = mfma(splat(sV1), trfrag(a1 + 32768, a1 + 33024), cs);
    cs = mfma(splat(up ? 0u : sV0), trfrag(d0, d0 + 256), cs);
    cs = mfma(splat(up ? 0u : sV1), trfrag(d1, d1 + 256), cs);
    *(LAS float*)(L_CSP + lane * 32 + wv * 4) = cs[0];
  }
  BARRIER();
  asm volatile("" : "+s"(tag));
  tag += 1;
  STAMP(1);
  if (wv == 7 && lane < 2) gst2(__builtin_bit_cast(u32x2, tag), lane ? WS_FA2 + (t1 * 16 + hd) * 8 : WS_FA1 + (hd * 16 + t1) * 8, rS, 0);
  bf16x8 ph0, ph1;
  { const unsigned a = L_PHI + (4 * q + qq) * 56, b0 = a + 8 * p, b1 = a + (p < 3 ? 32 + 8 * p : 24);
    u32x4 u0 = __builtin_bit_cast(u32x4, trfrag(b0, b0 + 896)), u1 = __builtin_bit_cast(u32x4, trfrag(b1, b1 + 896));
    u0[2] = pad ? 0u : u0[2]; u0[3] = pad ? 0u : u0[3]; u1[2] = pad ? 0u : u1[2]; u1[3] = pad ? 0u : u1[3];
    ph0 = asb(u0); ph1 = asb(u1);
    const f32x4 c = ldsf4(L_CSP + lane * 32) + ldsf4(L_CSP + lane * 32 + 16);
    *(LAS float*)(L_CSW + wv * 256 + lane * 4) = (c[0] + c[1]) + (c[2] + c[3]);
  }
  const unsigned wrow = (8 * q + qq) * 64, aW0 = L_W + wrow + oW0, aW1 = L_W + wrow + oW1;
  const unsigned aX = (r >> 3) * 8192 + wv * 1024 + (r & 7) * 128 + ((q ^ (((r >> 1) & 3) << 1)) << 4);
  const unsigned fo1 = r * 8;
  u32x4 xf[14]; u32x2 fl;
  f32x4 aK0 = zero4, aK1 = zero4, aV0 = zero4, aV1 = zero4, aQ = zero4;
  UNR for (int kp = 0; kp < 7; kp++) {
    bf16x8 wf[2][4];
    UNR for (int e = 0; e < 2; e++) { const unsigned o = (2 * kp + e) * 2048;
      wf[e][0] = trfrag(aW0 + o, aW0 + o + 256); wf[e][1] = trfrag(aW1 + o, aW1 + o + 256);
      wf[e][2] = trfrag(aW0 + o + 28672, aW0 + o + 28928); wf[e][3] = trfrag(aW1 + o + 28672, aW1 + o + 28928); }
    constexpr int wn[7] = {10, 10, 9, 9, 8, 7, 5};
    WAITV(wn[kp]);
    const unsigned xo = (kp % 6) * 16384;
    xf[2 * kp] = ldsu4(aX + xo); xf[2 * kp + 1] = ldsu4((aX ^ 64) + xo);
    UNR for (int e = 0; e < 2; e++) { const bf16x8 xb = asb(xf[2 * kp + e]);
      aQ = mfma(xb, xb, aQ); aK1 = mfma(wf[e][1], xb, aK1); aK0 = mfma(wf[e][0], xb, aK0); aV0 = mfma(wf[e][2], xb, aV0); aV1 = mfma(wf[e][3], xb, aV1); }
    USED(xf[2 * kp]); USED(xf[2 * kp + 1]);
    if (kp == 0) { glds(wv * 1024, v896, rX, xs + 768); glds((8 + wv) * 1024, v896, rX, xs + 7168 + 768); }
    if (kp == 1) glds(L_WQ + wv * 1024, vW, rW, wv * 43008 + wwin);
    if (kp == 2) { glds(L_WQ + (8 + wv) * 1024, vW, rW, (8 + wv) * 43008 + wwin); glds(L_WQ + (16 + wv) * 1024, vW, rW, (16 + wv) * 43008 + wwin); }
    if (kp == 3) { if (wv < 4) glds(L_WQ + (24 + wv) * 1024, vW, rW, (24 + wv) * 43008 + wwin); else glds(L_WO + (wv - 4) * 1024, v896, rWO, (wv - 4) * 7168 + 128 * ct); }
    if (kp == 4) gld2(fl, fo1, rS, WS_FA1 + hd * 128);
  }
  STAMP(2);
  float rstd, nm;
  const float wup = pad ? 0.f : 1.f;
  { const unsigned ri = r & 3; float d = ri == 0 ? aQ[0] : ri == 1 ? aQ[1] : ri == 2 ? aQ[2] : aQ[3]; d = (q == (r >> 2)) ? d : 0.f;
    const float mean = bcast_q3(aK1[0]) * (1.f / 448.f), var = fmaxf(allq_sum(d) * (1.f / 448.f) - mean * mean, 0.f);
    rstd = __builtin_amdgcn_rsqf(var + 1e-5f); nm = -mean * rstd;
    const unsigned ac = L_CSW + wv * 256 + q * 16;
    aK0 = fma4(aK0, rstd, ldsf4(ac) * nm); aK1 = fma4(aK1, rstd, ldsf4(ac + 64) * nm); aV0 = fma4(aV0, rstd, ldsf4(ac + 128) * nm); aV1 = fma4(aV1, rstd, ldsf4(ac + 192) * nm);
    const unsigned a = wv * 1024 + r * 32 + ((q ^ (r >> 2)) << 3);
    *(LAS u32x2*)(a + 16384) = u32x2{pk2(lo2(aV0)), pk2(hi2(aV0))};
    *(LAS u32x2*)(a + 16896) = u32x2{pad ? 0x00003f80u : pk2(lo2(aV1)), pad ? 0u : pk2(hi2(aV1))};
    u32x4 P, N;
    featmap(aK0, aK1, ph0, ph1, wup, P, N);
    *(LAS u32x2*)(a) = u32x2{P[0], P[1]}; *(LAS u32x2*)(a + 512) = u32x2{P[2], P[3]};
    *(LAS u32x2*)(a + 8192) = u32x2{N[0], N[1]}; *(LAS u32x2*)(a + 8704) = u32x2{N[2], N[3]};
  }
  BARRIER();
  STAMP(3);
#define COLSUMQ() do { const unsigned a0 = L_WQ + aCp + oW0, a1 = L_WQ + aCp + oW1; const bool z = q >= 2 && wv >= 4; \
    f32x4 cs = mfma(splat(sK0), trfrag(a0, a0 + 256), zero4); cs = mfma(splat(sK1), trfrag(a1, a1 + 256), cs); \
    cs = mfma(splat(z ? 0u : sK0), trfrag(a0 + 16384, a0 + 16640), cs); cs = mfma(splat(z ? 0u : sK1), trfrag(a1 + 16384, a1 + 16640), cs); \
    *(LAS float*)(L_CSQ + lane * 32 + wv * 4) = cs[0]; } while (0)
  if (wv < 4) {
    const unsigned ab = (4 * q + qq) * 32 + ((p ^ q) << 3), aA = ab + (wv >> 1) * 8192, aB = ab + 16384 + (wv & 1) * 512;
    f32x4 k0 = zero4, k1 = zero4;
    UNR for (int kk = 0; kk < 4; kk++) { const bf16x8 b = trfrag(aB + kk * 2048, aB + kk * 2048 + 1024);
      k0 = mfma(trfrag(aA + kk * 2048, aA + kk * 2048 + 1024), b, k0); k1 = mfma(trfrag(aA + kk * 2048 + 512, aA + kk * 2048 + 1536), b, k1); }
    gst(pk8(k0, k1), l16, rS, kvslot);
    STAMP(4);
    UNR for (int m = 0; m < 7; m++) { const unsigned n = 4 + wv + 8 * m; glds(L_WO + n * 1024, v896, rWO, n * 7168 + 128 * ct); }
    WAITV(8);
    COLSUMQ();
  } else {
    WAITV(0);
    COLSUMQ();
    STAMP(4);
    UNR for (int m = 0; m < 6; m++) { const unsigned n = 4 + wv + 8 * m; glds(L_WO + n * 1024, v896, rWO, n * 7168 + 128 * ct); }
  }
  BARRIER();
  STAMP(5);
  asm volatile("" : "+v"(fl));
  FLAGS(fl, fo1, WS_FA1 + hd * 128);
  gld2(fl, fo1, rS, WS_FA2 + g * 128);
  u32x4 gv[8];
  const unsigned kvs = WS_KV + ((hd * 16 + 8 * kh) * 4 + tq) * 1024;
  UNR for (int i = 0; i < 8; i++) gld<0>(gv[i], l16, rS, kvs + i * 4096);
  const unsigned ttp = 8 * g + 4 * th + tq, hslot = WS_H + (ttp * 14 + 2 * ct + kh) * 1024, stslot = WS_ST + ttp * 2048 + (2 * ct + kh) * 128;
  const unsigned hhslot = WS_HH + ((8 * g + 2 * tq) * 56 + 4 * mm + 2 * kh) * 1024, vst = q == 0 ? r * 8 : OOB;
  gst(sent4, l16, rS2, hslot); gst2(u32x2{SENT, SENT}, vst, rS2, stslot);
  UNR for (int i = 0; i < 4; i++) gst(sent4, l16, rS2, hhslot + (i >> 1) * 57344 + (i & 1) * 1024);
  const unsigned w1s = tq * 8 * 3584 + 128 * kh + 256 * mm;
#define W1R(ks) glds(((ks) >= 7 ? 32768u : 0u) + (ks) * 8192 + wv * 1024, v3584, rW1, w1s + (ks) * 114688)
  W1R(10); W1R(11); W1R(12); W1R(13);
  u32x4 phqP, phqN;
  { const f32x4 c = ldsf4(L_CSQ + lane * 32) + ldsf4(L_CSQ + lane * 32 + 16);
    *(LAS float*)(L_CSW + wv * 256 + lane * 4) = (c[0] + c[1]) + (c[2] + c[3]);
    const unsigned a0 = L_WQ + wrow + oW0, a1 = L_WQ + wrow + oW1;
    f32x4 aQ0 = zero4, aQ1 = zero4;
    UNR for (int ks = 0; ks < 14; ks++) { const bf16x8 xb = asb(xf[ks]);
      aQ0 = mfma(trfrag(a0 + ks * 2048, a0 + ks * 2048 + 256), xb, aQ0); aQ1 = mfma(trfrag(a1 + ks * 2048, a1 + ks * 2048 + 256), xb, aQ1); }
    const unsigned ac = L_CSW + wv * 256 + q * 16;
    aQ0 = fma4(aQ0, rstd, ldsf4(ac) * nm); aQ1 = fma4(aQ1, rstd, ldsf4(ac + 64) * nm);
    featmap(aQ0, aQ1, ph0, ph1, wup, phqP, phqN);
  }
  STAMP(6);
  bf16x8 sel1, sel2;
  { const unsigned one = (r >> 2) == q ? ((r & 1) ? 0x3f800000u : 0x3f80u) : 0u, e0 = (r & 2) ? 0u : one, e1 = (r & 2) ? one : 0u;
    sel1 = asb(u32x4{e0, e1, 0u, 0u}); sel2 = asb(u32x4{0u, 0u, e0, e1}); }
  { f32x4 s0 = zero4, s1 = zero4;
    UNR for (int i = 0; i < 8; i++) { GOT(17 - i, gv[i], l16, kvs + i * 4096); s0 = mfma(sel1, asb(gv[i]), s0); s1 = mfma(sel2, asb(gv[i]), s1); }
    *(LAS u32x4*)(L_KVF + wv * 1024 + l16) = pk8(s0, s1);
  }
  BARRIER();
  STAMP(7);
  { f32x4 o0 = zero4, o1 = zero4;
    UNR for (int ph = 0; ph < 2; ph++) { const unsigned a = L_KVF + ph * 4096 + l16;
      o1 = mfma(asb(ldsu4(a + 1024)), asb(phqP), o1); o0 = mfma(asb(ldsu4(a)), asb(phqP), o0);
      o1 = mfma(asb(ldsu4(a + 3072)), asb(phqN), o1); o0 = mfma(asb(ldsu4(a + 2048)), asb(phqN), o0); }
    const float rd = __builtin_amdgcn_rcpf(bcast_q3(o1[0]) + 1e-6f);
    gst(pk8(o0 * rd, o1 * (rd * wup)), l16, rS, oslot);
  }
  STAMP(8);
  if (spare) return;
  W1R(0); W1R(1); W1R(2); W1R(3); W1R(4); W1R(5); W1R(6 + 3 * kh);
  asm volatile("" : "+v"(fl));
  FLAGS(fl, fo1, WS_FA2 + g * 128);
  const unsigned os = WS_O + (ttp * 16 + 8 * kh) * 1024;
  gld<0>(gv[0], l16, rS, os); gld<1024>(gv[1], l16, rS, os); gld<2048>(gv[2], l16, rS, os); gld<3072>(gv[3], l16, rS, os);
  gld<0>(gv[4], l16, rS, os + 4096); gld<1024>(gv[5], l16, rS, os + 4096); gld<2048>(gv[6], l16, rS, os + 4096); gld<3072>(gv[7], l16, rS, os + 4096);
  unsigned aT[4], aL[4];
  { const unsigned f = ((2 * q + (qq >> 1)) & 3) << 1;
    UNR for (int T = 0; T < 4; T++) { aT[T] = (4 * q + qq) * 128 + (((2 * T + (p >> 1)) ^ f) << 4) + (p & 1) * 8; aL[T] = aT[T] ^ (kh << 6); } }
  bf16x8 wof[8][4];
  { const unsigned b = L_WO + kh * 28672, s = pad ? 0u : 2048u;
    UNR for (int i = 0; i < 8; i++) UNR for (int L = 0; L < 4; L++) { const unsigned a = b + aL[L ^ ((i & 1) << 1)]; wof[i][L] = trfrag(a + i * 3584, a + s + i * 3584); }
    UNR for (int i = 0; i < 8; i++) UNR for (int L = 0; L < 4; L++) asm volatile("" : "+v"(wof[i][L])); }
  WAITV(20);
  BARRIER();
  STAMP(9);
  if (wv == 7 && lane == 0) gst2(__builtin_bit_cast(u32x2, tag), WS_FB + (g * 16 + mm) * 8, rS, 0);
  const unsigned stg = L_STG + wv * 4096;
  W1R(9 - 3 * kh); W1R(7);
  glds(stg, (lane >> 2) * 896 + (((lane & 3) ^ (lane >> 4)) << 4), rX, (128 * g + 64 * th + 16 * tq) * 896 + 128 * ct + 64 * kh);
  u32x2 b1r; gld2p(b1r, q * 8, mkrs(ka[10], ka[11], 3584), 256 * mm + 32 * wv);
  f32x4 h0, h1;
  const unsigned fob = min(r, 13u) * 8;
  { f32x4 acc[4] = {zero4, zero4, zero4, zero4};
    UNR for (int i = 0; i < 8; i++) {
      GOT(i == 0 ? 11 : 12 - i, gv[i], l16, os + i * 1024);
      if (i == 0) gld2(fl, fob, rS, WS_FB + g * 128);
      UNR for (int L = 3; L >= 0; L--) acc[L] = mfma(wof[i][L], asb(gv[i]), acc[L]);
    }
    STAMP(10);
    *(LAS f32x4*)(L_EX + wv * 2048 + l16) = acc[2]; *(LAS f32x4*)(L_EX + wv * 2048 + 1024 + l16) = acc[3];
    WAITV(2);
    const unsigned ax = stg + r * 64 + (q & 1) * 8;
    h0 = acc[0] + unp4(*(LAS u32x2*)(ax + (((q >> 1) ^ (r >> 2)) << 4))); h1 = acc[1] + unp4(*(LAS u32x2*)(ax + (((2 + (q >> 1)) ^ (r >> 2)) << 4)));
    BARRIER();
    h0 += ldsf4(L_EX + (wv ^ 4) * 2048 + l16); h1 += ldsf4(L_EX + (wv ^ 4) * 2048 + 1024 + l16);
    gst(pk8(h0, h1), l16, rS, hslot);
    const f32x4 s4 = h0 + h1, q4 = h0 * h0 + h1 * h1;
    const float s = allq_sum((s4[0] + s4[1]) + (s4[2] + s4[3])), sq = allq_sum((q4[0] + q4[1]) + (q4[2] + q4[3]));
    gst2(u32x2{f2u(s), f2u(sq)}, vst, rS, stslot);
  }
  STAMP(11);
  if (w == 0 && wv == 7 && lane == 0) gst2(__builtin_bit_cast(u32x2, tag), 0, rS, 0);
  W1R(8);
  const u32x4 rW2 = mkrs(ka[12], ka[13], 1792 * 896);
  const unsigned w2s = 224 * wv * 896 + 128 * ct;
#define W2R(rho, dst) do { UNR for (int e = 0; e < 4; e++) glds((dst) + e * 1024, v896, rW2, w2s + (32 * (rho) + 8 * e) * 896); } while (0)
#define W2C(rho, src) do { UNR for (int L = 0; L < 4; L++) w2f[rho][L] = trfrag((src) + aL[L], (src) + aL[L] + 2048); \
    UNR for (int L = 0; L < 4; L++) asm volatile("" : "+v"(w2f[rho][L])); } while (0)
  bf16x8 w2f[7][4];
  W2R(0, stg);
  WT(7, fl);
  FLAGS(fl, fob, WS_FB + g * 128);
  u32x4 hf[14], st0, st1;
  const unsigned hs = WS_H + ((8 * g + 2 * tq) * 14 + 7 * kh) * 1024;
  UNR for (int ks = 0; ks < 7; ks++) UNR for (int t = 0; t < 2; t++) {
    const unsigned so = hs + t * 14336 + (ks >> 2) * 4096;
    switch (ks & 3) { case 0: gld<0>(hf[2 * ks + t], l16, rS, so); break; case 1: gld<1024>(hf[2 * ks + t], l16, rS, so); break;
                      case 2: gld<2048>(hf[2 * ks + t], l16, rS, so); break; default: gld<3072>(hf[2 * ks + t], l16, rS, so); } }
  const unsigned sts = WS_ST + (8 * g + 2 * tq + kh) * 2048, vs1 = lane < 48 ? l16 : OOB;
  gld<0>(st0, l16, rS, sts); gld<1024>(st1, vs1, rS, sts);
  WT(20, b1r);
  BARRIER();
  STAMP(12);
  { f32x4 cs = zero4; const unsigned a = (aT[0] ^ (tq << 5)) + kh * 4096; const bf16x8 ones = splat(ONE2);
    UNR for (int ks = 0; ks < 7; ks++) cs = mfma(trfrag(a + ks * 8192, a + ks * 8192 + 2048), ones, cs);
    const unsigned a2 = a + 90112;
    UNR for (int ks = 0; ks < 7; ks++) cs = mfma(trfrag(a2 + ks * 8192, a2 + ks * 8192 + 2048), ones, cs);
    if (r == 0) { *(LAS f32x4*)(L_CS1 + wv * 128 + q * 32) = cs; *(LAS f32x4*)(L_CS1 + wv * 128 + q * 32 + 16) = unp4(b1r); } }
  WAITV(16); W2C(0, stg); W2R(1, stg);
  f32x4 acc[2][8];
  UNR for (int t = 0; t < 2; t++) UNR for (int L = 0; L < 8; L++) acc[t][L] = zero4;
  { const unsigned bk = kh * 90112 + kh * 4096, bs = kh * 90112 + (kh ^ 1) * 4096;
    unsigned aG[8];
    UNR for (int L = 0; L < 8; L++) aG[L] = aT[L & 3] + (L < 4 ? bk : bs);
    UNR for (int ks = 0; ks < 7; ks++) {
      bf16x8 af[8];
      UNR for (int L = 0; L < 8; L++) af[L] = trfrag(aG[L] + ks * 8192, aG[L] + ks * 8192 + 2048);
      GOT(19 - 2 * ks, hf[2 * ks], l16, hs + ks * 1024);
      UNR for (int L = 0; L < 8; L++) acc[0][L] = mfma(af[L], asb(hf[2 * ks]), acc[0][L]);
      GOT(18 - 2 * ks, hf[2 * ks + 1], l16, hs + 14336 + ks * 1024);
      UNR for (int L = 0; L < 8; L++) acc[1][L] = mfma(af[L], asb(hf[2 * ks + 1]), acc[1][L]);
    } }
  STAMP(13);
  GOT(5, st0, l16, sts); GOT(4, st1, vs1, sts + 1024);
  { float v[4];
    UNR for (int i = 0; i < 4; i++) { float a = u2f(st0[i]) + u2f(st1[i]); a += u2f(__builtin_amdgcn_update_dpp(0u, f2u(a), 0x128, 0xf, 0xf, false)); v[i] = allq_sum(a) * (1.f / 448.f); }
    f32x4 o;
    UNR for (int i = 0; i < 2; i++) { const float m = v[2 * i], var = fmaxf(v[2 * i + 1] - m * m, 0.f), r1 = __builtin_amdgcn_rsqf(var + 1e-5f);
      const float R = r1 * __builtin_amdgcn_rsqf(var * r1 * r1 + 1e-5f); o[2 * i] = R; o[2 * i + 1] = -m * R; }
    if (lane < 8) *(LAS f32x4*)(L_STAT + (2 * tq + kh) * 128 + l16) = o; }
  WAITV(0); W2C(1, stg);
  UNR for (int t = 0; t < 2; t++) UNR for (int jj = 0; jj < 2; jj++) *(LAS u32x4*)(stg + (2 * t + jj) * 1024 + l16) = pk8(acc[t][4 + 2 * jj], acc[t][5 + 2 * jj]);
  BARRIER();
  STAMP(14);
  const unsigned ck0 = wv * 4096, ck1 = 32768 + wv * 4096 + (wv >= 6 ? 32768 : 0), ck2 = 98304 + wv * 4096, ck3 = 131072 + wv * 4096;
  W2R(2, ck0); W2R(3, ck1); W2R(4, ck2);
  { const unsigned ps = L_STG + (wv ^ 4) * 4096 + l16;
    UNR for (int t = 0; t < 2; t++) UNR for (int jj = 0; jj < 2; jj++) { const bf16x8 b = asb(ldsu4(ps + (2 * t + jj) * 1024));
      acc[t][2 * jj] = mfma(sel1, b, acc[t][2 * jj]); acc[t][2 * jj + 1] = mfma(sel2, b, acc[t][2 * jj + 1]); }
    f32x2 rn[2];
    UNR for (int t = 0; t < 2; t++) rn[t] = *(LAS f32x2*)(L_STAT + (2 * tq + t) * 128 + r * 8);
    UNR for (int jj = 0; jj < 2; jj++) {
      f32x4 cs[2], bb[2];
      UNR for (int e = 0; e < 2; e++) { const unsigned a = L_CS1 + (4 * kh + 2 * jj + e) * 128 + q * 32; cs[e] = ldsf4(a); bb[e] = ldsf4(a + 16); }
      UNR for (int t = 0; t < 2; t++) { u32x4 gq;
        UNR for (int e = 0; e < 2; e++) { const f32x4 v = fma4(acc[t][2 * jj + e], rn[t][0], fma4(cs[e], rn[t][1], bb[e])); gq[2 * e] = gelu2(lo2(v)); gq[2 * e + 1] = gelu2(hi2(v)); }
        gst(gq, l16, rS, hhslot + t * 57344 + jj * 1024); }
    } }
  STAMP(15);
  BARRIER();
  W2R(5, ck3); W2R(6, stg);
  u32x4 b2r; gld4p(b2r, (lane & 7) * 16, mkrs(ka[14], ka[15], 896), 128 * ct);
  u32x4 gh[21];
  const unsigned hhs = WS_HH + ((8 * g + 4 * th) * 56 + 7 * wv) * 1024;
#define HHG(tp, base) do { const unsigned so = hhs + ((tp) ^ tq) * 57344; gld<0>(gh[base], l16, rS, so); gld<1024>(gh[base + 1], l16, rS, so); gld<2048>(gh[base + 2], l16, rS, so); \
    gld<3072>(gh[base + 3], l16, rS, so); gld<0>(gh[base + 4], l16, rS, so + 4096); gld<1024>(gh[base + 5], l16, rS, so + 4096); gld<2048>(gh[base + 6], l16, rS, so + 4096); } while (0)
  HHG(0, 0); HHG(1, 7); HHG(2, 14);
  WAITV(34); W2C(2, ck0); W2C(3, ck1); W2C(4, ck2);
  WT(21, b2r); W2C(5, ck3); W2C(6, stg);
  STAMP(16);
  UNR for (int tp = 0; tp < 4; tp++) {
    f32x4 a2[4] = {tp == 0 ? h0 : zero4, tp == 0 ? h1 : zero4, zero4, zero4};
    const unsigned t = tp ^ tq, so = hhs + t * 57344;
    UNR for (int i = 0; i < 7; i++) {
      GOT(6 - i + (tp < 2 ? 14 : tp == 2 ? 7 : 0), gh[(tp % 3) * 7 + i], l16, so + i * 1024);
      UNR for (int L = 0; L < 4; L++) a2[L] = mfma(w2f[i][L], asb(gh[(tp % 3) * 7 + i]), a2[L]);
    }
    if (tp == 0) HHG(3, 0);
    const unsigned ck = wv * 4096 + t * 32768 + (wv + 8 * t >= 14 ? 32768 : 0) + r * 256;
    UNR for (int L = 0; L < 4; L++) *(LAS f32x4*)(ck + (((4 * L + q) ^ (8 * kh) ^ r) << 4)) = a2[L];
  }
  STAMP(17);
  BARRIER();
  { const unsigned t = wv >> 1, tok = 8 * (wv & 1) + (lane >> 3), c8 = lane & 7;
    const unsigned b = t * 32768 + (t >= 2 ? 32768 : 0) + tok * 256, e0 = b + (((2 * c8) ^ tok) << 4), e1 = b + (((2 * c8 + 1) ^ tok) << 4), hi = t == 1 ? 32768 : 0;
    f32x4 s0 = unp4(u32x2{b2r[0], b2r[1]}), s1 = unp4(u32x2{b2r[2], b2r[3]});
    UNR for (int v = 0; v < 8; v++) { s0 += ldsf4(e0 + v * 4096 + (v >= 6 ? hi : 0)); s1 += ldsf4(e1 + v * 4096 + (v >= 6 ? hi : 0)); }
    gstp(pk8(s0, s1), (lane >> 3) * 896 + c8 * 16, mkrs(kb4[0], kb4[1], 2048 * 896), (128 * g + 64 * th + 8 * wv) * 896 + 128 * ct);
  }
  STAMP(NSTAMP - 1); CSTAMP(31);
}

extern "C" void kernel_launch(void* const* d_in, const int* in_sizes, int n_in, void* d_out, int out_size, void* d_ws, size_t ws_size, hipStream_t stream) {
  (void)hipFuncSetAttribute((const void*)fused_kernel, hipFuncAttributeMaxDynamicSharedMemorySize, LDS_BYTES);
  fused_kernel<<<256, 512, LDS_BYTES, stream>>>(d_in[0], d_in[1], d_in[2], d_in[3], d_in[5], d_in[6], d_in[7], d_in[8], d_out, d_ws);
}
```

```cpp
#include <hip/hip_runtime.h>
#include <hip/hip_bf16.h>

typedef unsigned short u16;
typedef unsigned long long u64;
typedef __bf16 bf16x2 __attribute__((ext_vector_type(2)));
typedef __bf16 bf16x4 __attribute__((ext_vector_type(4)));
typedef __bf16 bf16x8 __attribute__((ext_vector_type(8)));
typedef float f32x2 __attribute__((ext_vector_type(2)));
typedef float f32x4 __attribute__((ext_vector_type(4)));
typedef unsigned u32x2 __attribute__((ext_vector_type(2)));
typedef unsigned u32x4 __attribute__((ext_vector_type(4)));
typedef unsigned u32x16 __attribute__((ext_vector_type(16)));
#define LAS __attribute__((address_space(3)))
#define GAS __attribute__((address_space(1)))
#define DI __device__ __forceinline__
#define UNR _Pragma("unroll")

constexpr unsigned WS_FA1 = 4096, WS_FA2 = 8192, WS_FB = 12288;
constexpr unsigned WS_KV = 65536;
constexpr unsigned WS_O = WS_KV + (1u << 20);
constexpr unsigned WS_H = WS_O + (2u << 20);
constexpr unsigned WS_ST = WS_H + 128 * 14 * 1024;
constexpr unsigned WS_HH = WS_ST + 128 * 2048;
constexpr unsigned WS_END = WS_HH + 128 * 56 * 1024;
constexpr unsigned WS_DBG = 16u << 20;
constexpr unsigned L_WQ = 24576, L_WO = 53248, L_W = 98304, L_KVF = 147456, L_MISC = 155648;
constexpr unsigned L_PHI = L_MISC, L_CSP = L_MISC + 2048, L_CSW = L_MISC + 4096, L_CSQ = L_MISC + 6144;
constexpr unsigned L_STG = 57344, L_EX = 147456, L_CS1 = 147456, L_STAT = 148480, LDS_BYTES = 163840;
constexpr unsigned SENT = 0xFFFFFFFFu, OOB = 0xF0000000u, ONE2 = 0x3f803f80u;
constexpr int SPIN_CAP = 1 << 18;

DI unsigned rfl(unsigned v) { return __builtin_amdgcn_readfirstlane(v); }
DI u32x4 mkrs(unsigned lo, unsigned hi, unsigned bytes) { return u32x4{lo, hi & 0xffffu, bytes, 0x00020000u}; }
DI bf16x4 trd(unsigned a) { return __builtin_amdgcn_ds_read_tr16_b64_v4bf16((LAS bf16x4*)a); }
DI bf16x8 trfrag(unsigned a0, unsigned a1) { return __builtin_shufflevector(trd(a0), trd(a1), 0, 1, 2, 3, 4, 5, 6, 7); }
DI f32x4 mfma(bf16x8 a, bf16x8 b, f32x4 c) { return __builtin_amdgcn_mfma_f32_16x16x32_bf16(a, b, c, 0, 0, 0); }
DI unsigned pk2(f32x2 f) { return __builtin_bit_cast(unsigned, __builtin_convertvector(f, bf16x2)); }
DI unsigned pk2(float a, float b) { return pk2(f32x2{a, b}); }
DI u32x4 pk8(f32x4 a, f32x4 b) { return u32x4{pk2(a[0], a[1]), pk2(a[2], a[3]), pk2(b[0], b[1]), pk2(b[2], b[3])}; }
DI bf16x8 asb(u32x4 v) { return __builtin_bit_cast(bf16x8, v); }
DI bf16x8 splat(unsigned s) { return asb(u32x4{s, s, s, s}); }
DI float blo(unsigned u) { return __builtin_bit_cast(float, u << 16); }
DI float bhi(unsigned u) { return __builtin_bit_cast(float, u & 0xffff0000u); }
DI f32x4 unp4(u32x2 u) { return f32x4{blo(u[0]), bhi(u[0]), blo(u[1]), bhi(u[1])}; }
DI unsigned f2u(float f) { return __builtin_bit_cast(unsigned, f); }
DI float u2f(unsigned u) { return __builtin_bit_cast(float, u); }
DI f32x2 lo2(f32x4 v) { return f32x2{v[0], v[1]}; }
DI f32x2 hi2(f32x4 v) { return f32x2{v[2], v[3]}; }
DI f32x2 sp2(float v) { return f32x2{v, v}; }
DI f32x2 fma2(f32x2 a, f32x2 b, f32x2 c) { return __builtin_elementwise_fma(a, b, c); }
DI f32x4 fma4(f32x4 a, float b, f32x4 c) { return __builtin_elementwise_fma(a, f32x4{b, b, b, b}, c); }
DI float hadd(f32x2 v) { float a = v[0]; asm("" : "+v"(a)); return a + v[1]; }
DI float own(float a) { asm("" : "+v"(a)); return a; }
DI float allq_sum(float v) {
  auto a = __builtin_amdgcn_permlane32_swap(f2u(v), f2u(v), false, false); float t = u2f(a[0]) + u2f(a[1]);
  auto b = __builtin_amdgcn_permlane16_swap(f2u(t), f2u(t), false, false); return u2f(b[0]) + u2f(b[1]);
}
DI float allq_max(float v) {
  auto a = __builtin_amdgcn_permlane32_swap(f2u(v), f2u(v), false, false); float t = fmaxf(u2f(a[0]), u2f(a[1]));
  auto b = __builtin_amdgcn_permlane16_swap(f2u(t), f2u(t), false, false); return fmaxf(u2f(b[0]), u2f(b[1]));
}
DI float allq_min(float v) {
  auto a = __builtin_amdgcn_permlane32_swap(f2u(v), f2u(v), false, false); float t = fminf(u2f(a[0]), u2f(a[1]));
  auto b = __builtin_amdgcn_permlane16_swap(f2u(t), f2u(t), false, false); return fminf(u2f(b[0]), u2f(b[1]));
}
DI float bcast_q3(float v) {
  auto a = __builtin_amdgcn_permlane32_swap(f2u(v), f2u(v), false, false);
  auto b = __builtin_amdgcn_permlane16_swap(a[1], a[1], false, false); return u2f(b[1]);
}
DI unsigned umax3(unsigned a, unsigned b, unsigned c) { return max(max(a, b), c); }
DI bool bad4(u32x4 v) { return __builtin_amdgcn_ballot_w64(max(umax3(v.x, v.y, v.z), v.w) == SENT) != 0; }
DI bool badtag(u32x2 f, u64 tag) { return __builtin_amdgcn_ballot_w64(__builtin_bit_cast(u64, f) != tag) != 0; }
#define WAITV(n) asm volatile("s_waitcnt vmcnt(%0)" :: "i"(n) : "memory")
#define WT(n, v) asm volatile("s_waitcnt vmcnt(%1)" : "+v"(v) : "i"(n) : "memory")
#define BARRIER() asm volatile("s_waitcnt lgkmcnt(0)\n\ts_barrier" ::: "memory")
#define USED(v) asm volatile("" :: "v"(v) : "memory")
#define UNLIKELY(c) __builtin_expect((c), 0)
DI void glds(unsigned ldsa, unsigned voff, u32x4 rs, unsigned soff) {
  asm volatile("s_mov_b32 m0, %0\n\ts_nop 0\n\tbuffer_load_dwordx4 %1, %2, %3 offen lds" :: "s"(ldsa), "v"(voff), "s"(rs), "s"(soff) : "memory");
}
template <int OFF> DI void gld(u32x4& d, unsigned voff, u32x4 rs, unsigned soff) {
  asm volatile("buffer_load_dwordx4 %0, %1, %2, %3 offen offset:%4 sc1" : "=v"(d) : "v"(voff), "s"(rs), "s"(soff), "i"(OFF) : "memory");
}
DI void gld_again(u32x4& d, unsigned voff, u32x4 rs, unsigned soff) {
  asm volatile("s_sleep 1\n\tbuffer_load_dwordx4 %0, %1, %2, %3 offen sc1\n\ts_waitcnt vmcnt(0)" : "+v"(d) : "v"(voff), "s"(rs), "s"(soff) : "memory");
}
DI void gld2(u32x2& d, unsigned voff, u32x4 rs, unsigned soff) {
  asm volatile("buffer_load_dwordx2 %0, %1, %2, %3 offen sc1" : "=v"(d) : "v"(voff), "s"(rs), "s"(soff) : "memory");
}
DI void gld2_again(u32x2& d, unsigned voff, u32x4 rs, unsigned soff) {
  asm volatile("s_sleep 1\n\tbuffer_load_dwordx2 %0, %1, %2, %3 offen sc1\n\ts_waitcnt vmcnt(0)" : "+v"(d) : "v"(voff), "s"(rs), "s"(soff) : "memory");
}
DI void gld2p(u32x2& d, unsigned voff, u32x4 rs, unsigned soff) {
  asm volatile("buffer_load_dwordx2 %0, %1, %2, %3 offen" : "=v"(d) : "v"(voff), "s"(rs), "s"(soff) : "memory");
}
DI void gld4p(u32x4& d, unsigned voff, u32x4 rs, unsigned soff) {
  asm volatile("buffer_load_dwordx4 %0, %1, %2, %3 offen" : "=v"(d) : "v"(voff), "s"(rs), "s"(soff) : "memory");
}
DI void gst(u32x4 v, unsigned voff, u32x4 rs, unsigned soff) {
  asm volatile("buffer_store_dwordx4 %0, %1, %2, %3 offen sc1\n\ts_nop 1" :: "v"(v), "v"(voff), "s"(rs), "s"(soff) : "memory");
}
DI void gst2(u32x2 v, unsigned voff, u32x4 rs, unsigned soff) {
  asm volatile("buffer_store_dwordx2 %0, %1, %2, %3 offen sc1" :: "v"(v), "v"(voff), "s"(rs), "s"(soff) : "memory");
}
DI void gstp(u32x4 v, unsigned voff, u32x4 rs, unsigned soff) {
  asm volatile("buffer_store_dwordx4 %0, %1, %2, %3 offen\n\ts_nop 1" :: "v"(v), "v"(voff), "s"(rs), "s"(soff) : "memory");
}
DI f32x4 ldsf4(unsigned a) { return *(LAS f32x4*)a; }
DI u32x4 ldsu4(unsigned a) { return *(LAS u32x4*)a; }
#define GOT(n, v, voff, soff) do { WT(n, v); if (UNLIKELY(bad4(v))) { do gld_again(v, voff, rS, soff); while (bad4(v) && ++spins < SPIN_CAP); } } while (0)
#define FLAGS(f, voff, soff) do { if (UNLIKELY(badtag(f, tag))) { do gld2_again(f, voff, rS, soff); while (badtag(f, tag) && ++spins < SPIN_CAP); } } while (0)

#define NSTAMP 20
#define STAMP(k) do {} while (0)
#define CSTAMP(k) do {} while (0)

DI void featmap(const f32x4 a0, const f32x4 a1, const bf16x8 ph0, const bf16x8 ph1, const float wup, u32x4& P, u32x4& N) {
  const f32x4 z = {0.f, 0.f, 0.f, 0.f};
  const bf16x8 kb = asb(pk8(a0, a1));
  const f32x4 p0 = mfma(ph0, kb, z), p1 = mfma(ph1, kb, z);
  float mx = fmaxf(fmaxf(fmaxf(fmaxf(p0[0], p0[1]), p0[2]), fmaxf(fmaxf(p0[3], p1[0]), p1[1])), fmaxf(p1[2], p1[3]));
  float mn = fminf(fminf(fminf(fminf(p0[0], p0[1]), p0[2]), fminf(fminf(p0[3], p1[0]), p1[1])), fminf(p1[2], p1[3]));
  mx = allq_max(mx); mn = allq_min(mn);
  const float L2E = 1.4426950408889634f;
  const f32x2 cx = sp2(-mx * L2E), cn = sp2(mn * L2E), kp = sp2(L2E), kn = sp2(-L2E);
  const f32x2 pp[4] = {lo2(p0), hi2(p0), lo2(p1), hi2(p1)};
  f32x2 ep[4], en[4];
  UNR for (int i = 0; i < 4; i++) {
    const f32x2 u = fma2(pp[i], kp, cx), v = fma2(pp[i], kn, cn);
    ep[i] = f32x2{__builtin_amdgcn_exp2f(u[0]), __builtin_amdgcn_exp2f(u[1])};
    en[i] = f32x2{__builtin_amdgcn_exp2f(v[0]), __builtin_amdgcn_exp2f(v[1])};
  }
  const f32x2 s1 = fma2(ep[2] + ep[3], sp2(wup), ep[0] + ep[1]), s2 = fma2(en[2] + en[3], sp2(wup), en[0] + en[1]);
  const float sp = __builtin_amdgcn_rcpf(allq_sum(hadd(s1))), sn = __builtin_amdgcn_rcpf(allq_sum(hadd(s2)));
  const float spu = sp * wup, snu = sn * wup;
  P = u32x4{pk2(ep[0] * sp2(sp)), pk2(ep[1] * sp2(sp)), pk2(ep[2] * sp2(spu)), pk2(ep[3] * sp2(spu))};
  N = u32x4{pk2(en[0] * sp2(sn)), pk2(en[1] * sp2(sn)), pk2(en[2] * sp2(snu)), pk2(en[3] * sp2(snu))};
}
DI unsigned gelu2(f32x2 v) {
  const float C1 = -2.302208198f, C2 = C1 * 0.044715f;
  const f32x2 z = v * fma2(v * v, sp2(C2), sp2(C1));
  const f32x2 d = f32x2{__builtin_amdgcn_exp2f(z[0]), __builtin_amdgcn_exp2f(z[1])} + sp2(1.f);
  return pk2(v * f32x2{__builtin_amdgcn_rcpf(d[0]), __builtin_amdgcn_rcpf(d[1])});
}

__global__ void __launch_bounds__(512, 2) fused_kernel(const void* a_x, const void* a_wqkv, const void* a_wphi, const void* a_wo, const void* a_w1, const void* a_b1,
                                                       const void* a_w2, const void* a_b2, void* a_out, void* a_ws) {
  u32x16 ka; u32x4 kb4;
  asm volatile("s_load_dwordx16 %0, %2, 0x0\n\ts_load_dwordx4 %1, %2, 0x40" : "=&s"(ka), "=&s"(kb4) : "s"(__builtin_amdgcn_kernarg_segment_ptr()));
  const unsigned tid = threadIdx.x, lane = tid & 63, wv = rfl(tid >> 6);
  const unsigned q = lane >> 4, r = lane & 15, qq = (lane >> 2) & 3, p = lane & 3, l16 = lane * 16;
  const unsigned w = blockIdx.x, x8 = w & 7, ii = w >> 3, loc = ii & 7;
  const unsigned t1 = 4 * (x8 >> 1) + (ii >> 3), hd = 8 * (x8 & 1) + loc;
  const unsigned sp = (x8 & 1) ? 3 : 4;
  const bool spare = loc == sp;
  const unsigned i2 = (ii >> 3) * 7 + loc - (loc > sp), g = 2 * x8 + (i2 >= 14), mm = i2 - 14 * (i2 >= 14), th = mm >= 7, ct = mm - 7 * th;
  const unsigned tq = wv & 3, kh = wv >> 2;
  const bool pad = (q == 3);
  const unsigned vW = (lane >> 2) * 2688 + (((lane & 3) ^ ((lane >> 5) << 1)) << 4), wwin = (56 * hd) & ~15u;
  const unsigned sw8 = ((lane & 7) ^ ((lane >> 4) << 1)) << 4, v896 = (lane >> 3) * 896 + sw8, v3584 = (lane >> 3) * 3584 + sw8;
  const u32x4 sent4 = {SENT, SENT, SENT, SENT};
  const f32x4 zero4 = {0.f, 0.f, 0.f, 0.f};
  const unsigned kvslot = WS_KV + ((hd * 16 + t1) * 4 + wv) * 1024, oslot = WS_O + ((8 * t1 + wv) * 16 + hd) * 1024;
  const unsigned xs = (128 * t1 + 16 * wv) * 896;
  int spins = 0;
  asm volatile("s_waitcnt lgkmcnt(0)" : "+s"(ka), "+s"(kb4));
  u64 tag;
  { const u64 wsa = ((u64)kb4[3] << 32) | kb4[2]; asm volatile("s_load_dwordx2 %0, %1, 0x0" : "=s"(tag) : "s"(wsa)); }
  const u32x4 rS = mkrs(kb4[2], kb4[3], WS_END), rX = mkrs(ka[0], ka[1], 2048 * 896), rW = mkrs(ka[2], ka[3], 448 * 2688);
  STAMP(0); CSTAMP(30);
  if (wv < 4) gst(sent4, l16, rS, kvslot);
  gst(sent4, l16, rS, oslot);
  if (wv < 2) glds(L_PHI + wv * 1024, l16, mkrs(ka[4], ka[5], 16 * 1568), hd * 1568 + wv * 1024);
  UNR for (int m = 0; m < 7; m++) { const unsigned n = wv + 8 * m; glds(L_W + n * 1024, vW, rW, n * 43008 + 896 + wwin - (n >= 28 ? 28 * 43008 - 896 : 0)); }
  UNR for (int j = 0; j < 12; j++) glds((8 * j + wv) * 1024, v896, rX, xs + (j & 1) * 7168 + (j >> 1) * 128);
  const u32x4 rWO = mkrs(ka[6], ka[7], spare ? 0 : 448 * 896), rW1 = mkrs(ka[8], ka[9], spare ? 0 : 448 * 3584), rS2 = mkrs(kb4[2], kb4[3], spare ? 0 : WS_END);
  const unsigned sh = 8 * (hd & 1), padoff = (hd & 1) ? 0 : 56;
  const unsigned oW0 = (8 * p + sh) ^ ((q & 1) << 5), oW1 = (p < 3 ? 32 + 8 * p + sh : padoff) ^ ((q & 1) << 5);
  const unsigned aC = wv * 1024 + (8 * (q & 1) + qq) * 64, aCp = aC + (q >> 1) * 8192;
  const unsigned sK0 = r == 0 ? ONE2 : 0u, sK1 = r == 4 ? ONE2 : 0u;
  WAITV(12);
  *(LAS u32x2*)(L_W + (wv + 8 * q) * 1024 + r * 64 + (padoff ^ ((r >> 3) << 5))) = u32x2{ONE2, ONE2};
  { const unsigned sV0 = r == 8 ? ONE2 : 0u, sV1 = r == 12 ? ONE2 : 0u; const bool up = q >= 2, upv = up && wv >= 4;
    const unsigned a0 = L_W + aCp + oW0, a1 = L_W + aCp + oW1, d0 = L_W + 49152 + aC + oW0, d1 = L_W + 49152 + aC + oW1;
    f32x4 cs = mfma(splat(sK0), trfrag(a0, a0 + 256), zero4);
    cs = mfma(splat(sK1), trfrag(a1, a1 + 256), cs);
    cs = mfma(splat(upv ? sV0 : sK0), trfrag(a0 + 16384, a0 + 16640), cs);
    cs = mfma(splat(upv ? sV1 : sK1), trfrag(a1 + 16384, a1 + 16640), cs);
    cs = mfma(splat(sV0), trfrag(a0 + 32768, a0 + 33024), cs);
    cs = mfma(splat(sV1), trfrag(a1 + 32768, a1 + 33024), cs);
    cs = mfma(splat(up ? 0u : sV0), trfrag(d0, d0 + 256), cs);
    cs = mfma(splat(up ? 0u : sV1), trfrag(d1, d1 + 256), cs);
    *(LAS float*)(L_CSP + lane * 32 + wv * 4) = cs[0];
  }
  BARRIER();
  asm volatile("" : "+s"(tag));
  tag += 1;
  STAMP(1);
  if (wv == 7 && lane < 2) gst2(__builtin_bit_cast(u32x2, tag), lane ? WS_FA2 + (t1 * 16 + hd) * 8 : WS_FA1 + (hd * 16 + t1) * 8, rS, 0);
  bf16x8 ph0, ph1;
  { const unsigned a = L_PHI + (4 * q + qq) * 56, b0 = a + 8 * p, b1 = a + (p < 3 ? 32 + 8 * p : 24);
    u32x4 u0 = __builtin_bit_cast(u32x4, trfrag(b0, b0 + 896)), u1 = __builtin_bit_cast(u32x4, trfrag(b1, b1 + 896));
    u0[2] = pad ? 0u : u0[2]; u0[3] = pad ? 0u : u0[3]; u1[2] = pad ? 0u : u1[2]; u1[3] = pad ? 0u : u1[3];
    ph0 = asb(u0); ph1 = asb(u1);
    const f32x4 c = ldsf4(L_CSP + lane * 32) + ldsf4(L_CSP + lane * 32 + 16);
    *(LAS float*)(L_CSW + wv * 256 + lane * 4) = (c[0] + c[1]) + (c[2] + c[3]);
  }
  const unsigned wrow = (8 * q + qq) * 64, aW0 = L_W + wrow + oW0, aW1 = L_W + wrow + oW1;
  const unsigned aX = (r >> 3) * 8192 + wv * 1024 + (r & 7) * 128 + ((q ^ (((r >> 1) & 3) << 1)) << 4);
  const unsigned fo1 = r * 8;
  u32x4 xf[14]; u32x2 fl;
  f32x4 aK0 = zero4, aK1 = zero4, aV0 = zero4, aV1 = zero4, aQ = zero4;
  UNR for (int kp = 0; kp < 7; kp++) {
    bf16x8 wf[2][4];
    UNR for (int e = 0; e < 2; e++) { const unsigned o = (2 * kp + e) * 2048;
      wf[e][0] = trfrag(aW0 + o, aW0 + o + 256); wf[e][1] = trfrag(aW1 + o, aW1 + o + 256);
      wf[e][2] = trfrag(aW0 + o + 28672, aW0 + o + 28928); wf[e][3] = trfrag(aW1 + o + 28672, aW1 + o + 28928); }
    constexpr int wn[7] = {10, 10, 9, 9, 8, 7, 5};
    WAITV(wn[kp]);
    const unsigned xo = (kp % 6) * 16384;
    xf[2 * kp] = ldsu4(aX + xo); xf[2 * kp + 1] = ldsu4((aX ^ 64) + xo);
    UNR for (int e = 0; e < 2; e++) { const bf16x8 xb = asb(xf[2 * kp + e]);
      aQ = mfma(xb, xb, aQ); aK1 = mfma(wf[e][1], xb, aK1); aK0 = mfma(wf[e][0], xb, aK0); aV0 = mfma(wf[e][2], xb, aV0); aV1 = mfma(wf[e][3], xb, aV1); }
    USED(xf[2 * kp]); USED(xf[2 * kp + 1]);
    if (kp == 0) { glds(wv * 1024, v896, rX, xs + 768); glds((8 + wv) * 1024, v896, rX, xs + 7168 + 768); }
    if (kp == 1) glds(L_WQ + wv * 1024, vW, rW, wv * 43008 + wwin);
    if (kp == 2) { glds(L_WQ + (8 + wv) * 1024, vW, rW, (8 + wv) * 43008 + wwin); glds(L_WQ + (16 + wv) * 1024, vW, rW, (16 + wv) * 43008 + wwin); }
    if (kp == 3) { if (wv < 4) glds(L_WQ + (24 + wv) * 1024, vW, rW, (24 + wv) * 43008 + wwin); else glds(L_WO + (wv - 4) * 1024, v896, rWO, (wv - 4) * 7168 + 128 * ct); }
    if (kp == 4) gld2(fl, fo1, rS, WS_FA1 + hd * 128);
  }
  STAMP(2);
  float rstd, nm;
  const float wup = pad ? 0.f : 1.f;
  { const unsigned ri = r & 3; float d = ri == 0 ? aQ[0] : ri == 1 ? aQ[1] : ri == 2 ? aQ[2] : aQ[3]; d = (q == (r >> 2)) ? d : 0.f;
    const float mean = bcast_q3(aK1[0]) * (1.f / 448.f), var = fmaxf(allq_sum(d) * (1.f / 448.f) - mean * mean, 0.f);
    rstd = __builtin_amdgcn_rsqf(var + 1e-5f); nm = -mean * rstd;
    const unsigned ac = L_CSW + wv * 256 + q * 16;
    aK0 = fma4(aK0, rstd, ldsf4(ac) * nm); aK1 = fma4(aK1, rstd, ldsf4(ac + 64) * nm); aV0 = fma4(aV0, rstd, ldsf4(ac + 128) * nm); aV1 = fma4(aV1, rstd, ldsf4(ac + 192) * nm);
    const unsigned a = wv * 1024 + r * 32 + ((q ^ (r >> 2)) << 3);
    *(LAS u32x2*)(a + 16384) = u32x2{pk2(lo2(aV0)), pk2(hi2(aV0))};
    *(LAS u32x2*)(a + 16896) = u32x2{pad ? 0x00003f80u : pk2(lo2(aV1)), pad ? 0u : pk2(hi2(aV1))};
    u32x4 P, N;
    featmap(aK0, aK1, ph0, ph1, wup, P, N);
    *(LAS u32x2*)(a) = u32x2{P[0], P[1]}; *(LAS u32x2*)(a + 512) = u32x2{P[2], P[3]};
    *(LAS u32x2*)(a + 8192) = u32x2{N[0], N[1]}; *(LAS u32x2*)(a + 8704) = u32x2{N[2], N[3]};
  }
  BARRIER();
  STAMP(3);
#define COLSUMQ() do { const unsigned a0 = L_WQ + aCp + oW0, a1 = L_WQ + aCp + oW1; const bool z = q >= 2 && wv >= 4; \
    f32x4 cs = mfma(splat(sK0), trfrag(a0, a0 + 256), zero4); cs = mfma(splat(sK1), trfrag(a1, a1 + 256), cs); \
    cs = mfma(splat(z ? 0u : sK0), trfrag(a0 + 16384, a0 + 16640), cs); cs = mfma(splat(z ? 0u : sK1), trfrag(a1 + 16384, a1 + 16640), cs); \
    *(LAS float*)(L_CSQ + lane * 32 + wv * 4) = cs[0]; } while (0)
  if (wv < 4) {
    const unsigned ab = (4 * q + qq) * 32 + ((p ^ q) << 3), aA = ab + (wv >> 1) * 8192, aB = ab + 16384 + (wv & 1) * 512;
    f32x4 k0 = zero4, k1 = zero4;
    UNR for (int kk = 0; kk < 4; kk++) { const bf16x8 b = trfrag(aB + kk * 2048, aB + kk * 2048 + 1024);
      k0 = mfma(trfrag(aA + kk * 2048, aA + kk * 2048 + 1024), b, k0); k1 = mfma(trfrag(aA + kk * 2048 + 512, aA + kk * 2048 + 1536), b, k1); }
    gst(pk8(k0, k1), l16, rS, kvslot);
    STAMP(4);
    UNR for (int m = 0; m < 7; m++) { const unsigned n = 4 + wv + 8 * m; glds(L_WO + n * 1024, v896, rWO, n * 7168 + 128 * ct); }
    WAITV(8);
    COLSUMQ();
  } else {
    WAITV(0);
    COLSUMQ();
    STAMP(4);
    UNR for (int m = 0; m < 6; m++) { const unsigned n = 4 + wv + 8 * m; glds(L_WO + n * 1024, v896, rWO, n * 7168 + 128 * ct); }
  }
  BARRIER();
  STAMP(5);
  asm volatile("" : "+v"(fl));
  FLAGS(fl, fo1, WS_FA1 + hd * 128);
  gld2(fl, fo1, rS, WS_FA2 + g * 128);
  u32x4 gv[8];
  const unsigned kvs = WS_KV + ((hd * 16 + 8 * kh) * 4 + tq) * 1024;
  UNR for (int i = 0; i < 8; i++) gld<0>(gv[i], l16, rS, kvs + i * 4096);
  const unsigned ttp = 8 * g + 4 * th + tq, hslot = WS_H + (ttp * 14 + 2 * ct + kh) * 1024, stslot = WS_ST + ttp * 2048 + (2 * ct + kh) * 128;
  const unsigned hhslot = WS_HH + ((8 * g + 2 * tq) * 56 + 4 * mm + 2 * kh) * 1024, vst = q == 0 ? r * 8 : OOB;
  gst(sent4, l16, rS2, hslot); gst2(u32x2{SENT, SENT}, vst, rS2, stslot);
  UNR for (int i = 0; i < 4; i++) gst(sent4, l16, rS2, hhslot + (i >> 1) * 57344 + (i & 1) * 1024);
  const unsigned w1s = tq * 8 * 3584 + 128 * kh + 256 * mm;
#define W1R(ks) glds(((ks) >= 7 ? 32768u : 0u) + (ks) * 8192 + wv * 1024, v3584, rW1, w1s + (ks) * 114688)
  W1R(10); W1R(11); W1R(12); W1R(13);
  u32x4 phqP, phqN;
  { const f32x4 c = ldsf4(L_CSQ + lane * 32) + ldsf4(L_CSQ + lane * 32 + 16);
    *(LAS float*)(L_CSW + wv * 256 + lane * 4) = (c[0] + c[1]) + (c[2] + c[3]);
    const unsigned a0 = L_WQ + wrow + oW0, a1 = L_WQ + wrow + oW1;
    f32x4 aQ0 = zero4, aQ1 = zero4;
    UNR for (int ks = 0; ks < 14; ks++) { const bf16x8 xb = asb(xf[ks]);
      aQ0 = mfma(trfrag(a0 + ks * 2048, a0 + ks * 2048 + 256), xb, aQ0); aQ1 = mfma(trfrag(a1 + ks * 2048, a1 + ks * 2048 + 256), xb, aQ1); }
    const unsigned ac = L_CSW + wv * 256 + q * 16;
    aQ0 = fma4(aQ0, rstd, ldsf4(ac) * nm); aQ1 = fma4(aQ1, rstd, ldsf4(ac + 64) * nm);
    featmap(aQ0, aQ1, ph0, ph1, wup, phqP, phqN);
  }
  STAMP(6);
  bf16x8 sel1, sel2;
  { const unsigned one = (r >> 2) == q ? ((r & 1) ? 0x3f800000u : 0x3f80u) : 0u, e0 = (r & 2) ? 0u : one, e1 = (r & 2) ? one : 0u;
    sel1 = asb(u32x4{e0, e1, 0u, 0u}); sel2 = asb(u32x4{0u, 0u, e0, e1}); }
  { f32x4 s0 = zero4, s1 = zero4;
    UNR for (int i = 0; i < 8; i++) { GOT(17 - i, gv[i], l16, kvs + i * 4096); s0 = mfma(sel1, asb(gv[i]), s0); s1 = mfma(sel2, asb(gv[i]), s1); }
    *(LAS u32x4*)(L_KVF + wv * 1024 + l16) = pk8(s0, s1);
  }
  BARRIER();
  STAMP(7);
  { f32x4 o0 = zero4, o1 = zero4;
    UNR for (int ph = 0; ph < 2; ph++) { const unsigned a = L_KVF + ph * 4096 + l16;
      o1 = mfma(asb(ldsu4(a + 1024)), asb(phqP), o1); o0 = mfma(asb(ldsu4(a)), asb(phqP), o0);
      o1 = mfma(asb(ldsu4(a + 3072)), asb(phqN), o1); o0 = mfma(asb(ldsu4(a + 2048)), asb(phqN), o0); }
    const float rd = __builtin_amdgcn_rcpf(bcast_q3(o1[0]) + 1e-6f);
    gst(pk8(o0 * rd, o1 * (rd * wup)), l16, rS, oslot);
  }
  STAMP(8);
  if (spare) return;
  W1R(0); W1R(1); W1R(2); W1R(3); W1R(4); W1R(5); W1R(6 + 3 * kh);
  asm volatile("" : "+v"(fl));
  FLAGS(fl, fo1, WS_FA2 + g * 128);
  const unsigned os = WS_O + (ttp * 16 + 8 * kh) * 1024;
  gld<0>(gv[0], l16, rS, os); gld<1024>(gv[1], l16, rS, os); gld<2048>(gv[2], l16, rS, os); gld<3072>(gv[3], l16, rS, os);
  gld<0>(gv[4], l16, rS, os + 4096); gld<1024>(gv[5], l16, rS, os + 4096); gld<2048>(gv[6], l16, rS, os + 4096); gld<3072>(gv[7], l16, rS, os + 4096);
  unsigned aT[4], aL[4];
  { const unsigned f = ((2 * q + (qq >> 1)) & 3) << 1;
    UNR for (int T = 0; T < 4; T++) { aT[T] = (4 * q + qq) * 128 + (((2 * T + (p >> 1)) ^ f) << 4) + (p & 1) * 8; aL[T] = aT[T] ^ (kh << 6); } }
  bf16x8 wof[8][4];
  { const unsigned b = L_WO + kh * 28672, s = pad ? 0u : 2048u;
    UNR for (int i = 0; i < 8; i++) UNR for (int L = 0; L < 4; L++) { const unsigned a = b + aL[L ^ ((i & 1) << 1)]; wof[i][L] = trfrag(a + i * 3584, a + s + i * 3584); }
    UNR for (int i = 0; i < 8; i++) UNR for (int L = 0; L < 4; L++) asm volatile("" : "+v"(wof[i][L])); }
  WAITV(20);
  BARRIER();
  STAMP(9);
  if (wv == 7 && lane == 0) gst2(__builtin_bit_cast(u32x2, tag), WS_FB + (g * 16 + mm) * 8, rS, 0);
  const unsigned stg = L_STG + wv * 4096;
  W1R(9 - 3 * kh); W1R(7);
  glds(stg, (lane >> 2) * 896 + (((lane & 3) ^ (lane >> 4)) << 4), rX, (128 * g + 64 * th + 16 * tq) * 896 + 128 * ct + 64 * kh);
  u32x2 b1r; gld2p(b1r, q * 8, mkrs(ka[10], ka[11], 3584), 256 * mm + 32 * wv);
  f32x4 h0, h1;
  const unsigned fob = min(r, 13u) * 8;
  { f32x4 acc[4] = {zero4, zero4, zero4, zero4};
    UNR for (int i = 0; i < 8; i++) {
      GOT(i == 0 ? 11 : 12 - i, gv[i], l16, os + i * 1024);
      if (i == 0) gld2(fl, fob, rS, WS_FB + g * 128);
      UNR for (int L = 3; L >= 0; L--) acc[L] = mfma(wof[i][L], asb(gv[i]), acc[L]);
    }
    STAMP(10);
    *(LAS f32x4*)(L_EX + wv * 2048 + l16) = acc[2]; *(LAS f32x4*)(L_EX + wv * 2048 + 1024 + l16) = acc[3];
    WAITV(2);
    const unsigned ax = stg + r * 64 + (q & 1) * 8;
    h0 = acc[0] + unp4(*(LAS u32x2*)(ax + (((q >> 1) ^ (r >> 2)) << 4))); h1 = acc[1] + unp4(*(LAS u32x2*)(ax + (((2 + (q >> 1)) ^ (r >> 2)) << 4)));
    BARRIER();
    h0 += ldsf4(L_EX + (wv ^ 4) * 2048 + l16); h1 += ldsf4(L_EX + (wv ^ 4) * 2048 + 1024 + l16);
    gst(pk8(h0, h1), l16, rS, hslot);
    const f32x4 s4 = h0 + h1, q4 = h0 * h0 + h1 * h1;
    const float s = allq_sum(hadd(lo2(s4) + hi2(s4))), sq = allq_sum(hadd(lo2(q4) + hi2(q4)));
    gst2(u32x2{f2u(s), f2u(sq)}, vst, rS, stslot);
  }
  STAMP(11);
  if (w == 0 && wv == 7 && lane == 0) gst2(__builtin_bit_cast(u32x2, tag), 0, rS, 0);
  W1R(8);
  const u32x4 rW2 = mkrs(ka[12], ka[13], 1792 * 896);
  const unsigned w2s = 224 * wv * 896 + 128 * ct;
#define W2R(rho, dst) do { UNR for (int e = 0; e < 4; e++) glds((dst) + e * 1024, v896, rW2, w2s + (32 * (rho) + 8 * e) * 896); } while (0)
#define W2C(rho, src) do { UNR for (int L = 0; L < 4; L++) w2f[rho][L] = trfrag((src) + aL[L], (src) + aL[L] + 2048); \
    UNR for (int L = 0; L < 4; L++) asm volatile("" : "+v"(w2f[rho][L])); } while (0)
  bf16x8 w2f[7][4];
  W2R(0, stg);
  WT(7, fl);
  FLAGS(fl, fob, WS_FB + g * 128);
  u32x4 hf[14], st0, st1;
  const unsigned hs = WS_H + ((8 * g + 2 * tq) * 14 + 7 * kh) * 1024;
  UNR for (int ks = 0; ks < 7; ks++) UNR for (int t = 0; t < 2; t++) {
    const unsigned so = hs + t * 14336 + (ks >> 2) * 4096;
    switch (ks & 3) { case 0: gld<0>(hf[2 * ks + t], l16, rS, so); break; case 1: gld<1024>(hf[2 * ks + t], l16, rS, so); break;
                      case 2: gld<2048>(hf[2 * ks + t], l16, rS, so); break; default: gld<3072>(hf[2 * ks + t], l16, rS, so); } }
  const unsigned sts = WS_ST + (8 * g + 2 * tq + kh) * 2048, vs1 = lane < 48 ? l16 : OOB;
  gld<0>(st0, l16, rS, sts); gld<1024>(st1, vs1, rS, sts);
  WT(20, b1r);
  BARRIER();
  STAMP(12);
  { f32x4 cs = zero4; const unsigned a = (aT[0] ^ (tq << 5)) + kh * 4096; const bf16x8 ones = splat(ONE2);
    UNR for (int ks = 0; ks < 7; ks++) cs = mfma(trfrag(a + ks * 8192, a + ks * 8192 + 2048), ones, cs);
    const unsigned a2 = a + 90112;
    UNR for (int ks = 0; ks < 7; ks++) cs = mfma(trfrag(a2 + ks * 8192, a2 + ks * 8192 + 2048), ones, cs);
    if (r == 0) { *(LAS f32x4*)(L_CS1 + wv * 128 + q * 32) = cs; *(LAS f32x4*)(L_CS1 + wv * 128 + q * 32 + 16) = unp4(b1r); } }
  WAITV(16); W2C(0, stg); W2R(1, stg);
  f32x4 acc[2][8];
  UNR for (int t = 0; t < 2; t++) UNR for (int L = 0; L < 8; L++) acc[t][L] = zero4;
  { const unsigned bk = kh * 90112 + kh * 4096, bs = kh * 90112 + (kh ^ 1) * 4096;
    unsigned aG[8];
    UNR for (int L = 0; L < 8; L++) aG[L] = aT[L & 3] + (L < 4 ? bk : bs);
    UNR for (int ks = 0; ks < 7; ks++) {
      bf16x8 af[8];
      UNR for (int L = 0; L < 8; L++) af[L] = trfrag(aG[L] + ks * 8192, aG[L] + ks * 8192 + 2048);
      GOT(19 - 2 * ks, hf[2 * ks], l16, hs + ks * 1024);
      UNR for (int L = 0; L < 8; L++) acc[0][L] = mfma(af[L], asb(hf[2 * ks]), acc[0][L]);
      GOT(18 - 2 * ks, hf[2 * ks + 1], l16, hs + 14336 + ks * 1024);
      UNR for (int L = 0; L < 8; L++) acc[1][L] = mfma(af[L], asb(hf[2 * ks + 1]), acc[1][L]);
    } }
  STAMP(13);
  GOT(5, st0, l16, sts); GOT(4, st1, vs1, sts + 1024);
  { float v[4];
    UNR for (int i = 0; i < 4; i++) { float a = u2f(st0[i]) + u2f(st1[i]); a += u2f(__builtin_amdgcn_update_dpp(0u, f2u(a), 0x128, 0xf, 0xf, false)); v[i] = allq_sum(a) * (1.f / 448.f); }
    f32x4 o;
    UNR for (int i = 0; i < 2; i++) { const float m = v[2 * i], var = fmaxf(v[2 * i + 1] - m * m, 0.f), r1 = __builtin_amdgcn_rsqf(var + 1e-5f);
      const float R = r1 * __builtin_amdgcn_rsqf(var * r1 * r1 + 1e-5f); o[2 * i] = R; o[2 * i + 1] = -m * R; }
    if (lane < 8) *(LAS f32x4*)(L_STAT + (2 * tq + kh) * 128 + l16) = o; }
  WAITV(0); W2C(1, stg);
  UNR for (int t = 0; t < 2; t++) UNR for (int jj = 0; jj < 2; jj++) *(LAS u32x4*)(stg + (2 * t + jj) * 1024 + l16) = pk8(acc[t][4 + 2 * jj], acc[t][5 + 2 * jj]);
  BARRIER();
  STAMP(14);
  const unsigned ck0 = wv * 4096, ck1 = 32768 + wv * 4096 + (wv >= 6 ? 32768 : 0), ck2 = 98304 + wv * 4096, ck3 = 131072 + wv * 4096;
  W2R(2, ck0); W2R(3, ck1); W2R(4, ck2);
  { const unsigned ps = L_STG + (wv ^ 4) * 4096 + l16;
    UNR for (int t = 0; t < 2; t++) UNR for (int jj = 0; jj < 2; jj++) { const bf16x8 b = asb(ldsu4(ps + (2 * t + jj) * 1024));
      acc[t][2 * jj] = mfma(sel1, b, acc[t][2 * jj]); acc[t][2 * jj + 1] = mfma(sel2, b, acc[t][2 * jj + 1]); }
    f32x2 rn[2];
    UNR for (int t = 0; t < 2; t++) rn[t] = *(LAS f32x2*)(L_STAT + (2 * tq + t) * 128 + r * 8);
    const float nmv[2] = {own(rn[0][1]), own(rn[1][1])};
    UNR for (int jj = 0; jj < 2; jj++) {
      f32x4 cs[2], bb[2];
      UNR for (int e = 0; e < 2; e++) { const unsigned a = L_CS1 + (4 * kh + 2 * jj + e) * 128 + q * 32; cs[e] = ldsf4(a); bb[e] = ldsf4(a + 16); }
      UNR for (int t = 0; t < 2; t++) { u32x4 gq;
        UNR for (int e = 0; e < 2; e++) { const f32x4 v = fma4(acc[t][2 * jj + e], rn[t][0], fma4(cs[e], nmv[t], bb[e])); gq[2 * e] = gelu2(lo2(v)); gq[2 * e + 1] = gelu2(hi2(v)); }
        gst(gq, l16, rS, hhslot + t * 57344 + jj * 1024); }
    } }
  STAMP(15);
  BARRIER();
  W2R(5, ck3); W2R(6, stg);
  u32x4 b2r; gld4p(b2r, (lane & 7) * 16, mkrs(ka[14], ka[15], 896), 128 * ct);
  u32x4 gh[21];
  const unsigned hhs = WS_HH + ((8 * g + 4 * th) * 56 + 7 * wv) * 1024;
#define HHG(tp, base) do { const unsigned so = hhs + ((tp) ^ tq) * 57344; gld<0>(gh[base], l16, rS, so); gld<1024>(gh[base + 1], l16, rS, so); gld<2048>(gh[base + 2], l16, rS, so); \
    gld<3072>(gh[base + 3], l16, rS, so); gld<0>(gh[base + 4], l16, rS, so + 4096); gld<1024>(gh[base + 5], l16, rS, so + 4096); gld<2048>(gh[base + 6], l16, rS, so + 4096); } while (0)
  HHG(0, 0); HHG(1, 7); HHG(2, 14);
  WAITV(34); W2C(2, ck0); W2C(3, ck1); W2C(4, ck2);
  WT(21, b2r); W2C(5, ck3); W2C(6, stg);
  STAMP(16);
  UNR for (int tp = 0; tp < 4; tp++) {
    f32x4 a2[4] = {tp == 0 ? h0 : zero4, tp == 0 ? h1 : zero4, zero4, zero4};
    const unsigned t = tp ^ tq, so = hhs + t * 57344;
    UNR for (int i = 0; i < 7; i++) {
      GOT(6 - i + (tp < 2 ? 14 : tp == 2 ? 7 : 0), gh[(tp % 3) * 7 + i], l16, so + i * 1024);
      UNR for (int L = 0; L < 4; L++) a2[L] = mfma(w2f[i][L], asb(gh[(tp % 3) * 7 + i]), a2[L]);
    }
    if (tp == 0) HHG(3, 0);
    const unsigned ck = wv * 4096 + t * 32768 + (wv + 8 * t >= 14 ? 32768 : 0) + r * 256;
    UNR for (int L = 0; L < 4; L++) *(LAS f32x4*)(ck + (((4 * L + q) ^ (8 * kh) ^ r) << 4)) = a2[L];
  }
  STAMP(17);
  BARRIER();
  { const unsigned t = wv >> 1, tok = 8 * (wv & 1) + (lane >> 3), c8 = lane & 7;
    const unsigned b = t * 32768 + (t >= 2 ? 32768 : 0) + tok * 256, e0 = b + (((2 * c8) ^ tok) << 4), e1 = b + (((2 * c8 + 1) ^ tok) << 4), hi = t == 1 ? 32768 : 0;
    f32x4 s0 = unp4(u32x2{b2r[0], b2r[1]}), s1 = unp4(u32x2{b2r[2], b2r[3]});
    UNR for (int v = 0; v < 8; v++) { s0 += ldsf4(e0 + v * 4096 + (v >= 6 ? hi : 0)); s1 += ldsf4(e1 + v * 4096 + (v >= 6 ? hi : 0)); }
    gstp(pk8(s0, s1), (lane >> 3) * 896 + c8 * 16, mkrs(kb4[0], kb4[1], 2048 * 896), (128 * g + 64 * th + 8 * wv) * 896 + 128 * ct);
  }
  STAMP(NSTAMP - 1); CSTAMP(31);
}

extern "C" void kernel_launch(void* const* d_in, const int* in_sizes, int n_in, void* d_out, int out_size, void* d_ws, size_t ws_size, hipStream_t stream) {
  (void)hipFuncSetAttribute((const void*)fused_kernel, hipFuncAttributeMaxDynamicSharedMemorySize, LDS_BYTES);
  fused_kernel<<<256, 512, LDS_BYTES, stream>>>(d_in[0], d_in[1], d_in[2], d_in[3], d_in[5], d_in[6], d_in[7], d_in[8], d_out, d_ws);
}
```

```cpp
#define PF2
#define PFO
#define PFC
#define PFS 0
#define PFT 0
#define PFU 0
#define SP 2
#define FPO
#define B3
#define NTAB 12, 20, 28, 35, 42, 42, 45
#define W1M 0
#define NG0 2
#define GI 2
#define W2E 1
#define GD 3
#define TM 1
#define PF2D 10
#define D2 6
#define XP 2
#define XPE 2
#define SC 14
#define SK 0
#define SL 11
#define XF 0
#define CKS 2, 6, 10
#define NC 3
#define OB 1
#define LAY 2
#define QD 3
#define JR 2
#define OPOL 2
#define WE 6
#define NTS 14, 26, 28, 35, 42, 42, 45
#include <hip/hip_runtime.h>
#include <hip/hip_bf16.h>

typedef unsigned short u16;
typedef unsigned long long u64;
typedef __bf16 bf16x2 __attribute__((ext_vector_type(2)));
typedef __bf16 bf16x4 __attribute__((ext_vector_type(4)));
typedef __bf16 bf16x8 __attribute__((ext_vector_type(8)));
typedef float f32x2 __attribute__((ext_vector_type(2)));
typedef float f32x4 __attribute__((ext_vector_type(4)));
typedef unsigned u32x2 __attribute__((ext_vector_type(2)));
typedef unsigned u32x4 __attribute__((ext_vector_type(4)));
typedef unsigned u32x16 __attribute__((ext_vector_type(16)));
#define LAS __attribute__((address_space(3)))
#define GAS __attribute__((address_space(1)))
#define DI __device__ __forceinline__
#define UNR _Pragma("unroll")

constexpr unsigned WS_FA1 = 4096, WS_FA2 = 8192, WS_FA3 = 12288;
constexpr unsigned WS_KV = 65536;
constexpr unsigned WS_O = WS_KV + (1u << 20);
#define WS_GROUPED
constexpr unsigned GS = 576 * 1024;
constexpr unsigned WS_HH = WS_O + (2u << 20);
constexpr unsigned WS_H = WS_HH + 448 * 1024;
constexpr unsigned WS_ST = WS_H + 112 * 1024;
constexpr unsigned WS_END = WS_HH + 16 * GS;
constexpr unsigned WS_DBG = 16u << 20;
constexpr unsigned L_WQ = 65536, L_WO = 98304, L_W = 98304, L_MISC = 155648;
constexpr unsigned L_PHI = L_MISC, L_ONE = L_MISC + 2048, L_CW = L_MISC + 3072, L_KVF = L_MISC + 4096;
constexpr unsigned L_STG = 114688, L_EX = 147456, L_CS1 = 147456, L_STAT = 148480, LDS_BYTES = 163840;
constexpr unsigned SENT = 0xFFFFFFFFu, OOB = 0xF0000000u, ONE2 = 0x3f803f80u;
constexpr int SPIN_CAP = 1 << 18;
constexpr int ordw(int i, int e) { return i < 12 || i >= 12 + 3 * e ? i : i < 12 + e ? 12 + 6 * ((i - 12) / 2) + (i - 12) % 2 : 14 + 6 * ((i - 12 - e) / 4) + (i - 12 - e) % 4; }
constexpr int needw(int kp, int e) { return kp < 2 || kp >= 2 + e / 2 ? 6 * kp + 6 : 8 + e + 4 * kp; }
#define WF 0
#define AHEAD 9
#define HP 0
#define EW 0
#define WOS 14
#define ZW1 1
#define ZWO 1
#define ZW2 1
#define PE 4
#define PK0 0
#define PKN 1
#define FL 0
#define NX 0
#define ER 0
#define ERP 0
#define PLM ""
#define PL 0
#define XE 0
#define XJ 4
#define XJL 0
#define WI 0
#define XW 0
#define RL 0
#define R2 0
#define AB 0
#define PA 0
#define PS 0
#define GKS 0, 2, 9
#define XP0 0
#define PF1 0
#define PF1P 0
#define PF1D 0
#define PFB 0
#define AB1 0
#define AB2 0
#define JQ 0
#define W2D 0
struct G1 {
  static constexpr int CK[NC + 1] = {CKS, 99};
  static constexpr int pre(int j) { return j <= GD ? 0 : (NG0 * (j - GD) + 3 - GD) / (4 - GD); }
  static constexpr int got(int ks) { return NG0 + GI * ks < 14 ? NG0 + GI * ks : 14; }
  static constexpr int poi(int ks) { return ks <= PK0 ? PE : PE + PKN * (ks - PK0) < 4 ? PE + PKN * (ks - PK0) : 4; }
  static constexpr int mid(int ks) { return cnt(ks) + (1 + AB2) * (got(ks + 1) - got(ks)) + poi(ks + 1) - poi(ks) + (ks == SK) + 4 * (ks == SL); }
  static constexpr int cnt(int ks) { int n = 4 + AB2 * (got(ks) - NG0) + got(ks) + poi(ks) - PE + (ks > SK) + 4 * (ks > SL); for (int c = 0; c < NC; c++) n += 4 * (ks > CK[c] + (RL > 0)); return n; }
  static constexpr int ih(int e) { if (e < NG0) return e + (W2E ? 0 : 4); int ks = 0; while (got(ks + 1) <= e) ks++; return cnt(ks) + (1 + AB2) * (e - got(ks)) + AB2; }
  static constexpr int iw(int c) { return c ? mid(CK[c - 1] + (RL > 0)) : W2E ? NG0 : 0; }
  static constexpr int is() { return mid(SL) - 1; }
  static constexpr int ip() { if (PE == 4) return -1; int ks = 0; while (poi(ks + 1) < 4) ks++; return cnt(ks) + got(ks + 1) - got(ks) + 3 - poi(ks); }
};
static_assert((ER == 0 || GD >= 2) && (ER < 2 || NX), "");
static_assert(G1::poi(SK) == 4, "all poison stores ahead of step SK");

DI unsigned rfl(unsigned v) { return __builtin_amdgcn_readfirstlane(v); }
DI u32x4 mkrs(unsigned lo, unsigned hi, unsigned bytes) { return u32x4{lo, hi & 0xffffu, bytes, 0x00020000u}; }
DI bf16x4 trd(unsigned a) { return __builtin_amdgcn_ds_read_tr16_b64_v4bf16((LAS bf16x4*)a); }
DI bf16x8 trfrag(unsigned a0, unsigned a1) { return __builtin_shufflevector(trd(a0), trd(a1), 0, 1, 2, 3, 4, 5, 6, 7); }
DI f32x4 mfma(bf16x8 a, bf16x8 b, f32x4 c) { return __builtin_amdgcn_mfma_f32_16x16x32_bf16(a, b, c, 0, 0, 0); }
DI unsigned pk2(f32x2 f) { return __builtin_bit_cast(unsigned, __builtin_convertvector(f, bf16x2)); }
DI unsigned pk2(float a, float b) { return pk2(f32x2{a, b}); }
DI u32x4 pk8(f32x4 a, f32x4 b) { return u32x4{pk2(a[0], a[1]), pk2(a[2], a[3]), pk2(b[0], b[1]), pk2(b[2], b[3])}; }
DI bf16x8 asb(u32x4 v) { return __builtin_bit_cast(bf16x8, v); }
DI bf16x8 splat(unsigned s) { return asb(u32x4{s, s, s, s}); }
DI float blo(unsigned u) { return __builtin_bit_cast(float, u << 16); }
DI float bhi(unsigned u) { return __builtin_bit_cast(float, u & 0xffff0000u); }
DI f32x4 unp4(u32x2 u) { return f32x4{blo(u[0]), bhi(u[0]), blo(u[1]), bhi(u[1])}; }
DI unsigned f2u(float f) { return __builtin_bit_cast(unsigned, f); }
DI float u2f(unsigned u) { return __builtin_bit_cast(float, u); }
DI f32x2 lo2(f32x4 v) { return f32x2{v[0], v[1]}; }
DI f32x2 hi2(f32x4 v) { return f32x2{v[2], v[3]}; }
DI f32x2 sp2(float v) { return f32x2{v, v}; }
DI f32x2 fma2(f32x2 a, f32x2 b, f32x2 c) { return __builtin_elementwise_fma(a, b, c); }
DI f32x4 fma4(f32x4 a, float b, f32x4 c) { return __builtin_elementwise_fma(a, f32x4{b, b, b, b}, c); }
DI float hadd(f32x2 v) { float a = v[0]; asm("" : "+v"(a)); return a + v[1]; }
DI float own(float a) { asm("" : "+v"(a)); return a; }
DI float allq_sum(float v) {
  auto a = __builtin_amdgcn_permlane32_swap(f2u(v), f2u(v), false, false); float t = u2f(a[0]) + u2f(a[1]);
  auto b = __builtin_amdgcn_permlane16_swap(f2u(t), f2u(t), false, false); return u2f(b[0]) + u2f(b[1]);
}
DI float allq_max(float v) {
  auto a = __builtin_amdgcn_permlane32_swap(f2u(v), f2u(v), false, false); float t = fmaxf(u2f(a[0]), u2f(a[1]));
  auto b = __builtin_amdgcn_permlane16_swap(f2u(t), f2u(t), false, false); return fmaxf(u2f(b[0]), u2f(b[1]));
}
DI float allq_min(float v) {
  auto a = __builtin_amdgcn_permlane32_swap(f2u(v), f2u(v), false, false); float t = fminf(u2f(a[0]), u2f(a[1]));
  auto b = __builtin_amdgcn_permlane16_swap(f2u(t), f2u(t), false, false); return fminf(u2f(b[0]), u2f(b[1]));
}
DI float bcast_q3(float v) {
  auto a = __builtin_amdgcn_permlane32_swap(f2u(v), f2u(v), false, false);
  auto b = __builtin_amdgcn_permlane16_swap(a[1], a[1], false, false); return u2f(b[1]);
}
DI unsigned umax3(unsigned a, unsigned b, unsigned c) { return max(max(a, b), c); }
DI bool bad4(u32x4 v) { return __builtin_amdgcn_ballot_w64(max(umax3(v.x, v.y, v.z), v.w) == SENT) != 0; }
DI u64 badm(u32x4 v) { u64 m = __builtin_amdgcn_ballot_w64(max(umax3(v.x, v.y, v.z), v.w) == SENT); asm volatile("" : "+s"(m)); return m; }
DI bool badtag(u32x2 f, u64 tag) { return __builtin_amdgcn_ballot_w64(__builtin_bit_cast(u64, f) != tag) != 0; }
#define WAITV(n) asm volatile("s_waitcnt vmcnt(%0)" :: "i"(n) : "memory")
#define WT(n, v) asm volatile("s_waitcnt vmcnt(%1)" : "+v"(v) : "i"(n) : "memory")
#define BARRIER() asm volatile("s_waitcnt lgkmcnt(0)\n\ts_barrier" ::: "memory")
#define SBAR() asm volatile("s_barrier" ::: "memory")
#define USED(v) asm volatile("" :: "v"(v) : "memory")
#define UNLIKELY(c) __builtin_expect((c), 0)
DI void glds(unsigned ldsa, unsigned voff, u32x4 rs, unsigned soff) {
  asm volatile("s_mov_b32 m0, %0\n\ts_nop 0\n\tbuffer_load_dwordx4 %1, %2, %3 offen lds" :: "s"(ldsa), "v"(voff), "s"(rs), "s"(soff) : "memory");
}
DI void pfl(unsigned ldsa, unsigned voff, u32x4 rs) {
  asm volatile("s_mov_b32 m0, %0\n\ts_nop 0\n\tbuffer_load_dword %1, %2, 0 offen lds" :: "s"(ldsa), "v"(voff), "s"(rs) : "memory");
}
template <int OFF> DI void gld(u32x4& d, unsigned voff, u32x4 rs, unsigned soff) {
  asm volatile("buffer_load_dwordx4 %0, %1, %2, %3 offen offset:%4 sc1" : "=v"(d) : "v"(voff), "s"(rs), "s"(soff), "i"(OFF) : "memory");
}
DI void gld_again(u32x4& d, unsigned voff, u32x4 rs, unsigned soff) {
  asm volatile("s_sleep 1\n\tbuffer_load_dwordx4 %0, %1, %2, %3 offen sc1\n\ts_waitcnt vmcnt(0)" : "+v"(d) : "v"(voff), "s"(rs), "s"(soff) : "memory");
}
DI void gld2(u32x2& d, unsigned voff, u32x4 rs, unsigned soff) {
  asm volatile("buffer_load_dwordx2 %0, %1, %2, %3 offen sc1" : "=v"(d) : "v"(voff), "s"(rs), "s"(soff) : "memory");
}
DI void gld2_again(u32x2& d, unsigned voff, u32x4 rs, unsigned soff) {
  asm volatile("s_sleep 1\n\tbuffer_load_dwordx2 %0, %1, %2, %3 offen sc1\n\ts_waitcnt vmcnt(0)" : "+v"(d) : "v"(voff), "s"(rs), "s"(soff) : "memory");
}
template <int OFF> DI void gld2o(u32x2& d, unsigned voff, u32x4 rs, unsigned soff) {
  asm volatile("buffer_load_dwordx2 %0, %1, %2, %3 offen offset:%4 sc1" : "=v"(d) : "v"(voff), "s"(rs), "s"(soff), "i"(OFF) : "memory");
}
DI void gld2_again4(u32x2 (&d)[4], unsigned v0, unsigned v1, u32x4 rs, unsigned soff) {
  asm volatile("s_sleep 1\n\tbuffer_load_dwordx2 %0, %4, %6, %7 offen sc1\n\tbuffer_load_dwordx2 %1, %4, %6, %7 offen offset:512 sc1\n\tbuffer_load_dwordx2 %2, %4, %6, %7 offen offset:1024 sc1\n\t"
               "buffer_load_dwordx2 %3, %5, %6, %7 offen offset:1536 sc1\n\ts_waitcnt vmcnt(0)" : "+v"(d[0]), "+v"(d[1]), "+v"(d[2]), "+v"(d[3]) : "v"(v0), "v"(v1), "s"(rs), "s"(soff) : "memory");
}
DI bool bad8(const u32x2 (&d)[4]) { return __builtin_amdgcn_ballot_w64(umax3(umax3(umax3(d[0].x, d[0].y, d[1].x), d[1].y, d[2].x), umax3(d[2].y, d[3].x, d[3].y), 0u) == SENT) != 0; }
DI void gld2p(u32x2& d, unsigned voff, u32x4 rs, unsigned soff) {
  asm volatile("s_nop 4\n\tbuffer_load_dwordx2 %0, %1, %2, %3 offen" : "=v"(d) : "v"(voff), "s"(rs), "s"(soff) : "memory");
}
template <int OFF> DI void gld2w(u32x2& d, unsigned voff, u32x4 rs, unsigned soff) {
  asm volatile("buffer_load_dwordx2 %0, %1, %2, %3 offen offset:%4" : "=v"(d) : "v"(voff), "s"(rs), "s"(soff), "i"(OFF) : "memory");
}
DI void gld4p(u32x4& d, unsigned voff, u32x4 rs, unsigned soff) {
  asm volatile("buffer_load_dwordx4 %0, %1, %2, %3 offen" : "=v"(d) : "v"(voff), "s"(rs), "s"(soff) : "memory");
}
DI void gst(u32x4 v, unsigned voff, u32x4 rs, unsigned soff) {
  asm volatile("buffer_store_dwordx4 %0, %1, %2, %3 offen sc1\n\ts_nop 1" :: "v"(v), "v"(voff), "s"(rs), "s"(soff) : "memory");
}
DI void gst2(u32x2 v, unsigned voff, u32x4 rs, unsigned soff) {
  asm volatile("buffer_store_dwordx2 %0, %1, %2, %3 offen sc1" :: "v"(v), "v"(voff), "s"(rs), "s"(soff) : "memory");
}
#define OPS " sc0 sc1"
DI void gstp(u32x4 v, unsigned voff, u32x4 rs, unsigned soff) {
  asm volatile("buffer_store_dwordx4 %0, %1, %2, %3 offen" OPS "\n\ts_nop 1" :: "v"(v), "v"(voff), "s"(rs), "s"(soff) : "memory");
}
DI f32x4 ldsf4(unsigned a) { return *(LAS f32x4*)a; }
DI u32x4 ldsu4(unsigned a) { return *(LAS u32x4*)a; }
template <int OFF> DI void gldr(u32x4& d, unsigned voff, u32x4 rs, unsigned soff) {
  asm volatile("buffer_load_dwordx4 %0, %1, %2, %3 offen offset:%4 sc1" : "+v"(d) : "v"(voff), "s"(rs), "s"(soff), "i"(OFF) : "memory");
}
template <int OFF> DI void gldn(u32x4& d, unsigned voff, u32x4 rs, unsigned soff) {
  asm volatile("buffer_load_dwordx4 %0, %1, %2, %3 offen offset:%4 " PLM : "=v"(d) : "v"(voff), "s"(rs), "s"(soff), "i"(OFF) : "memory");
}
DI void gldno(u32x4& d, unsigned voff, u32x4 rs, unsigned soff, int o) {
  const unsigned so = soff + (o >> 2) * 4096;
  switch (o & 3) { case 0: gldn<0>(d, voff, rs, so); break; case 1: gldn<1024>(d, voff, rs, so); break; case 2: gldn<2048>(d, voff, rs, so); break; default: gldn<3072>(d, voff, rs, so); }
}
template <bool AGAIN> DI void gldo(u32x4& d, unsigned voff, u32x4 rs, unsigned soff, int o) {
  const unsigned so = soff + (o >> 2) * 4096;
  if (AGAIN) switch (o & 3) { case 0: gldr<0>(d, voff, rs, so); break; case 1: gldr<1024>(d, voff, rs, so); break; case 2: gldr<2048>(d, voff, rs, so); break; default: gldr<3072>(d, voff, rs, so); }
  else switch (o & 3) { case 0: gld<0>(d, voff, rs, so); break; case 1: gld<1024>(d, voff, rs, so); break; case 2: gld<2048>(d, voff, rs, so); break; default: gld<3072>(d, voff, rs, so); }
}
#define GOTB(n, v, RELOAD) do { WT(n, v); if (UNLIKELY(bad4(v))) { do { RELOAD; WT(0, v); } while (bad4(v) && ++spins < SPIN_CAP); } } while (0)
#define GOT(n, v, voff, soff) do { WT(n, v); if (UNLIKELY(bad4(v))) { do gld_again(v, voff, rS, soff); while (bad4(v) && ++spins < SPIN_CAP); } } while (0)
#define AGAIN(m, v, voff, soff) do { if (UNLIKELY(m != 0)) { do gld_again(v, voff, rS, soff); while (bad4(v) && ++spins < SPIN_CAP); } } while (0)
#define FLAGS(f, voff, soff) do { if (UNLIKELY(badtag(f, tag))) { do gld2_again(f, voff, rS, soff); while (badtag(f, tag) && ++spins < SPIN_CAP); } } while (0)

#define NSTAMP 30
#define STAMP(k) do {} while (0)
#define CSTAMP(k) do {} while (0)
#define SFLUSH() do {} while (0)

#define SSTAMP(k) do {} while (0)
#define CST(k) do {} while (0)
#define KST(k) do {} while (0)
#define ESTAMP(k) STAMP(k)
#define TSTAMP(k) do {} while (0)
#define GSTAMP(k) do {} while (0)
#define LST(k) do {} while (0)
#define LSP(k) do {} while (0)
DI void sw32(float& a, float& b) { auto s = __builtin_amdgcn_permlane32_swap(f2u(a), f2u(b), false, false); a = u2f(s[0]); b = u2f(s[1]); }
DI void sw16(float& a, float& b) { auto s = __builtin_amdgcn_permlane16_swap(f2u(a), f2u(b), false, false); a = u2f(s[0]); b = u2f(s[1]); }
DI float mx2(float a, float b, float inf) { return __builtin_amdgcn_fmed3f(a, b, inf); }
DI float mx3(float a, float b, float c) { return fmaxf(fmaxf(a, b), c); }
DI float mn3(float a, float b, float c) { return fminf(fminf(a, b), c); }
DI float sum4(float a, float b, float c, float d) { sw32(a, b); sw32(c, d); float t = a + b, u = c + d; sw16(t, u); return t + u; }
DI float max4(float a, float b, float c, float d, float inf) { sw32(a, b); sw32(c, d); float t = mx2(a, b, inf), u = mx2(c, d, inf); sw16(t, u); return mx2(t, u, inf); }
DI void spl32(float x, float& lo, float& hi) { lo = x; hi = x; sw32(lo, hi); }
DI void spl16(float x, float& e, float& o) { e = x; o = x; sw16(e, o); }
DI void featmap2(const f32x4 (&a0)[2], const f32x4 (&a1)[2], const bf16x8 ph0, const bf16x8 ph1, const float rla, const float (&rl)[2], const float wup, const float inf,
                 u32x4 (&P)[2], u32x4 (&N)[2]) {
  const f32x4 z = {0.f, 0.f, 0.f, 0.f};
  f32x4 p0[2], p1[2];
  float mx[2], mn[2], cx[2], cn[2], t0, t1, sp[2], sn[2];
  UNR for (int u = 0; u < 2; u++) { const bf16x8 kb = asb(pk8(a0[u], a1[u])); p0[u] = mfma(ph0, kb, z); p1[u] = mfma(ph1, kb, z); }
  UNR for (int u = 0; u < 2; u++) {
    mx[u] = mx2(mx3(mx3(p0[u][0], p0[u][1], p0[u][2]), p0[u][3], p1[u][0]), mx3(p1[u][1], p1[u][2], p1[u][3]), inf);
    mn[u] = mx2(-mn3(mn3(p0[u][0], p0[u][1], p0[u][2]), p0[u][3], p1[u][0]), -mn3(p1[u][1], p1[u][2], p1[u][3]), inf);
  }
  spl32(max4(mx[0], mn[0], mx[1], mn[1], inf) * -rla, t0, t1); spl16(t0, cx[0], cx[1]); spl16(t1, cn[0], cn[1]);
  f32x2 ep[2][4], en[2][4];
  UNR for (int u = 0; u < 2; u++) {
    const f32x2 pp[4] = {lo2(p0[u]), hi2(p0[u]), lo2(p1[u]), hi2(p1[u])};
    UNR for (int i = 0; i < 4; i++) {
      const f32x2 a = fma2(pp[i], sp2(rl[u]), sp2(cx[u])), b = fma2(pp[i], sp2(-rl[u]), sp2(cn[u]));
      ep[u][i] = f32x2{__builtin_amdgcn_exp2f(a[0]), __builtin_amdgcn_exp2f(a[1])};
      en[u][i] = f32x2{__builtin_amdgcn_exp2f(b[0]), __builtin_amdgcn_exp2f(b[1])};
    }
    sp[u] = hadd(fma2(ep[u][2] + ep[u][3], sp2(wup), ep[u][0] + ep[u][1])); sn[u] = hadd(fma2(en[u][2] + en[u][3], sp2(wup), en[u][0] + en[u][1]));
  }
  spl32(__builtin_amdgcn_rcpf(sum4(sp[0], sn[0], sp[1], sn[1])), t0, t1); spl16(t0, sp[0], sp[1]); spl16(t1, sn[0], sn[1]);
  UNR for (int u = 0; u < 2; u++) {
    const float spu = sp[u] * wup, snu = sn[u] * wup;
    P[u] = u32x4{pk2(ep[u][0] * sp2(sp[u])), pk2(ep[u][1] * sp2(sp[u])), pk2(ep[u][2] * sp2(spu)), pk2(ep[u][3] * sp2(spu))};
    N[u] = u32x4{pk2(en[u][0] * sp2(sn[u])), pk2(en[u][1] * sp2(sn[u])), pk2(en[u][2] * sp2(snu)), pk2(en[u][3] * sp2(snu))};
  }
}
DI unsigned gelu2(f32x2 v) {
  const float C1 = -2.302208198f, C2 = C1 * 0.044715f;
  const f32x2 z = v * fma2(v * v, sp2(C2), sp2(C1));
  const f32x2 d = f32x2{__builtin_amdgcn_exp2f(z[0]), __builtin_amdgcn_exp2f(z[1])} + sp2(1.f);
  return pk2(v * f32x2{__builtin_amdgcn_rcpf(d[0]), __builtin_amdgcn_rcpf(d[1])});
}

__global__ void __launch_bounds__(512, 2) fused_kernel(const void* a_x, const void* a_wqkv, const void* a_wphi, const void* a_wo, const void* a_w1, const void* a_b1,
                                                       const void* a_w2, const void* a_b2, void* a_out, void* a_ws) {
  u32x16 ka; u32x4 kb4;
  SSTAMP(24);
  asm volatile("s_load_dwordx16 %0, %2, 0x0\n\ts_load_dwordx4 %1, %2, 0x40" : "=&s"(ka), "=&s"(kb4) : "s"(__builtin_amdgcn_kernarg_segment_ptr()));
  constexpr int NT[8] = {NTAB, 49};
  unsigned tid = threadIdx.x, w = blockIdx.x;
  {
    const unsigned lane = tid & 63, wv = rfl(tid >> 6), x8 = w & 7, ii = w >> 3, t1 = 4 * (x8 >> 1) + (ii >> 3), hd = 8 * (x8 & 1) + (ii & 7);
    const unsigned vW = (lane >> 2) * 2688 + (((lane & 3) ^ ((lane >> 5) << 1)) << 4), v896 = (lane >> 3) * 896 + (((lane & 7) ^ ((lane >> 4) << 1)) << 4);
    const unsigned wls = wv * 43008 + ((56 * hd) & ~15u), wld = L_W + wv * 1024, xs = (128 * t1 + 32 * wv) * 896;
    USED(vW); USED(v896); asm volatile("" :: "s"(wls), "s"(wld), "s"(xs));
    asm volatile("s_waitcnt lgkmcnt(0)" : "+s"(ka), "+s"(kb4));
    if (wv < 4) {
      const u32x4 rX = mkrs(ka[0], ka[1], 2048 * 896), rW = mkrs(ka[2], ka[3], 448 * 2688);
      constexpr int NU[8] = {NTS, 49};
      UNR for (int i = 0; i < 12; i++) {
        const int s = i / 6, j = i % 6;
        if (j < 2) glds(wld + j * 28672 + s * 4096, vW, rW, wls + 896 * (j + 1) + s * 172032);
        else glds((s & 3) * 16384 + (4 * wv + j - 2) * 1024, v896, rX, xs + (j - 2) * 7168 + s * 128);
      }
      if ((0xD5ABu >> hd) & 1) {
      UNR for (int i = 12; i < NT[0]; i++) {
        const int s = ordw(i, WF) / 6, j = ordw(i, WF) % 6;
        if (j < 2) glds(wld + j * 28672 + s * 4096, vW, rW, wls + 896 * (j + 1) + s * 172032);
        else glds((s & 3) * 16384 + (4 * wv + j - 2) * 1024, v896, rX, xs + (j - 2) * 7168 + s * 128);
      }
      } else {
      UNR for (int i = 12; i < NU[0]; i++) {
        const int s = ordw(i, WE) / 6, j = ordw(i, WE) % 6;
        if (j < 2) glds(wld + j * 28672 + s * 4096, vW, rW, wls + 896 * (j + 1) + s * 172032);
        else glds((s & 3) * 16384 + (4 * wv + j - 2) * 1024, v896, rX, xs + (j - 2) * 7168 + s * 128);
      }
      }
    }
    unsigned c = wv - 4;
    asm volatile("" : "+s"(c));
    if (c < 4) {
      const unsigned A = (0x01010107u >> (8 * c)) & 255, B = (0x0415071Cu >> (8 * c)) & 255, C = (0x082A1538u >> (8 * c)) & 255, D = (unsigned)(0x0010005405400001ull >> (16 * c)) & 0xffffu;
      const unsigned E = (unsigned)(0x000C0126002A0070ull >> (16 * c)) & 0xffffu, F0 = 3584 * (x8 >> 1) + 2, F1 = 3 * (x8 & 1) + 7 + 2 * 1344, F2 = 3 * (x8 & 1);
      const unsigned a = (E * ii + (c == 0 ? F0 : c == 1 ? F1 : c == 2 ? F2 : 0) + (lane & 3) * A + ((lane >> 2) & 1) * B + ((lane >> 3) & 1) * C + (lane >> 4) * D) << 7;
      u64 pc; unsigned n; asm volatile("s_getpc_b64 %0\nfk_from:\n\ts_mov_b32 %1, fk_to-fk_from" : "=s"(pc), "=s"(n));
      const u32x4 rp = mkrs(c == 0 ? ka[0] : c < 3 ? ka[2] : (unsigned)pc, c == 0 ? ka[1] : c < 3 ? ka[3] : (unsigned)(pc >> 32), c == 0 ? 2048 * 896 : c < 3 ? 448 * 2688 : n);
      pfl(L_PHI + 1792, lane < ((0x0C382020u >> (8 * c)) & 255) ? a : OOB, rp);
      if (c < 2) { pfl(L_PHI + 1792, lane < 32 ? a + D * 256 : OOB, rp); pfl(L_PHI + 1792, lane < 16 ? a + D * 512 : OOB, rp); }
    }
    asm volatile("" : "+v"(tid), "+s"(w) :: "memory");
  }
  const unsigned lane = tid & 63, wv = rfl(tid >> 6);
  const unsigned q = lane >> 4, r = lane & 15, qq = (lane >> 2) & 3, p = lane & 3, l16 = lane * 16;
  const unsigned x8 = w & 7, ii = w >> 3, loc = ii & 7;
  const unsigned t1 = 4 * (x8 >> 1) + (ii >> 3), hd = 8 * (x8 & 1) + loc;
  const unsigned sp = (x8 & 1) ? 3 : 4;
  const bool spare = loc == sp;
  const unsigned i2 = (ii >> 3) * 7 + loc - (loc > sp), g = 2 * x8 + (i2 >= 14), mm = i2 - 14 * (i2 >= 14), th = mm >= 7, ct = mm - 7 * th;
  const unsigned tq = wv & 3, kh = wv >> 2;
  const bool pad = (q == 3);
  const unsigned vW = (lane >> 2) * 2688 + (((lane & 3) ^ ((lane >> 5) << 1)) << 4), wwin = (56 * hd) & ~15u;
  const unsigned sw8 = ((lane & 7) ^ ((lane >> 4) << 1)) << 4, v896 = (lane >> 3) * 896 + sw8, v3584 = (lane >> 3) * 3584 + sw8;
  const u32x4 sent4 = {SENT, SENT, SENT, SENT};
  const f32x4 zero4 = {0.f, 0.f, 0.f, 0.f};
  int spins = 0;
  u64 tag;
  { const u64 wsa = ((u64)kb4[3] << 32) | kb4[2]; asm volatile("s_load_dwordx2 %0, %1, 0x0" : "=s"(tag) : "s"(wsa)); }
  const u32x4 rS = mkrs(kb4[2], kb4[3], WS_END), rX = mkrs(ka[0], ka[1], 2048 * 896);
  const u32x4 rW1 = mkrs(ka[8], ka[9], spare ? 0 : ZW1 * 448 * 3584);
  const unsigned fo1 = r * 8;
  const unsigned ttp = 8 * g + 4 * th + tq, hslot = WS_H + g * GS + ((4 * th + tq) * 14 + 2 * ct + kh) * 1024, stslot = WS_ST + g * GS + (4 * th + tq) * 2048 + (2 * ct + kh) * 128;
  const unsigned tw = TM ? (wv & 1) | ((wv & 2) << 1) | ((wv & 4) >> 1) : wv;
  const unsigned hhslot = WS_HH + g * GS + (tw * 56 + 4 * mm) * 1024, vst = q == 0 ? r * 8 : OOB;
  const unsigned w1s = tq * 8 * 3584 + 256 * mm;
#define W1P(ks, k2) if (ZW1) glds((ks) * 8192 + (tq + 4 * (k2)) * 1024, v3584, rW1, w1s + 128 * (k2) + (ks) * 114688)
#define W1R(ks) W1P(ks, kh)
  const unsigned sh = 8 * (hd & 1), padoff = (hd & 1) ? 0 : 56;
  const unsigned oW0 = (8 * p + sh) ^ ((q & 1) << 5), oW1 = (p < 3 ? 32 + 8 * p + sh : padoff) ^ ((q & 1) << 5), wrow = (8 * q + qq) * 64;
  const bf16x8 ones = splat(ONE2);
  STAMP(0);
  CSTAMP(30);
  if (wv < 4) {
    const u32x4 rW = mkrs(ka[2], ka[3], 448 * 2688);
    const unsigned wls = wv * 43008 + wwin, wld = L_W + wv * 1024, xs = (128 * t1 + 32 * wv) * 896;
    const unsigned kvslot = WS_KV + ((hd * 16 + t1) * 4 + wv) * 1024, oslot = WS_O + ((8 * t1 + 2 * wv) * 16 + hd) * 1024;
    constexpr int KF = NT[SP] / 6;
    u32x2 fl;
    const unsigned aC = L_W + wrow + ((wv & 1) ? oW1 : oW0) + (wv >> 1) * 28672;
    f32x4 cw = zero4;
    if ((wwin & 127) > 64) {
    constexpr int NT[8] = {NTS, 49}, WX = WE;
    static_assert(WX % 2 == 0 && WX <= 6 && NT[1] <= 26 && NT[2] <= 32 && NT[3] <= 38 && needw(KF - 1, WX) <= NT[SP] && needw(4, WX) == 30, "");
    static_assert(NT[0] >= 12 && NT[SP] / 6 == KF && NT[0] >= needw(0, WX) && NT[1] >= needw(1, WX) && NT[2] >= needw(2, WX) && NT[3] >= needw(3, WX), "");
    UNR for (int kp = 0; kp < 8; kp++) {
      const int n0 = kp == 0 ? (LAY >= 2 ? NT[0] : 0) : NT[kp - 1], n1 = NT[kp];
      bf16x8 cf[4];
      if (kp > 0 && kp < 7) UNR for (int h = 0; h < (kp == 6 ? 4 : 2); h++) cf[h] = trfrag(aC + (2 * kp - 2 + h) * 2048, aC + (2 * kp - 2 + h) * 2048 + 256);
      UNR for (int i = n0; i < n1; i++) {
        const int s = ordw(i, WX) / 6, j = ordw(i, WX) % 6;
        if (i >= 42) glds(L_WQ + wv * 1024 + (i - 42) * 4096, vW, rW, wls + (i - 42) * 172032);
        else if (j < 2) glds(wld + j * 28672 + s * 4096, vW, rW, wls + 896 * (j + 1) + s * 172032);
        else glds((s & 3) * 16384 + (4 * wv + j - 2) * 1024, v896, rX, xs + (j - 2) * 7168 + s * 128);
      }
      if (kp == SP) {
        gst(sent4, l16, rS, kvslot); gst(sent4, l16, rS, oslot); gst(sent4, l16, rS, oslot + 16384);
        if (wv < 2) glds(L_PHI + wv * 1024, l16, mkrs(ka[4], ka[5], 16 * 1568), hd * 1568 + wv * 1024);
      }
      if (kp == 0) STAMP(1);
      KST(12 + kp);
      if (kp == 7) break;
      if (kp >= 4) SSTAMP(1 + kp);
      if (kp > 0) UNR for (int h = 0; h < (kp == 6 ? 4 : 2); h++) cw = mfma(cf[h], ones, cw);
      if (kp == 6 && r == 0) *(LAS f32x4*)(L_CW + wv * 64 + q * 16) = cw;
      WAITV(n1 - needw(kp, WX) + (kp >= SP && kp < KF ? 3 : 0) - (kp == 5 ? 2 : 0));
      KST(4 + kp);
      if (kp == 0) ESTAMP(2);
      if (kp >= 4) SSTAMP(10 + kp);
      if (kp == 0 || kp == 6) BARRIER(); else SBAR();
      if (kp == 0) STAMP(3);
      if (kp == KF) {
        asm volatile("" : "+s"(tag));
        if (wv == 3 && lane < 2) gst2(__builtin_bit_cast(u32x2, tag + 1), lane ? WS_FA2 + (t1 * 16 + hd) * 8 : WS_FA1 + (hd * 16 + t1) * 8, rS, 0);
      }
    }
    } else {
    constexpr int WX = WF;
    static_assert(WX % 2 == 0 && WX <= 6 && NT[1] <= 26 && NT[2] <= 32 && NT[3] <= 38 && needw(KF - 1, WX) <= NT[SP] && needw(4, WX) == 30, "");
    static_assert(NT[0] >= 12 && NT[SP] / 6 == KF && NT[0] >= needw(0, WX) && NT[1] >= needw(1, WX) && NT[2] >= needw(2, WX) && NT[3] >= needw(3, WX), "");
    UNR for (int kp = 0; kp < 8; kp++) {
      const int n0 = kp == 0 ? (LAY >= 2 ? NT[0] : 0) : NT[kp - 1], n1 = NT[kp];
      bf16x8 cf[4];
      if (kp > 0 && kp < 7) UNR for (int h = 0; h < (kp == 6 ? 4 : 2); h++) cf[h] = trfrag(aC + (2 * kp - 2 + h) * 2048, aC + (2 * kp - 2 + h) * 2048 + 256);
      UNR for (int i = n0; i < n1; i++) {
        const int s = ordw(i, WX) / 6, j = ordw(i, WX) % 6;
        if (i >= 42) glds(L_WQ + wv * 1024 + (i - 42) * 4096, vW, rW, wls + (i - 42) * 172032);
        else if (j < 2) glds(wld + j * 28672 + s * 4096, vW, rW, wls + 896 * (j + 1) + s * 172032);
        else glds((s & 3) * 16384 + (4 * wv + j - 2) * 1024, v896, rX, xs + (j - 2) * 7168 + s * 128);
      }
      if (kp == SP) {
        gst(sent4, l16, rS, kvslot); gst(sent4, l16, rS, oslot); gst(sent4, l16, rS, oslot + 16384);
        if (wv < 2) glds(L_PHI + wv * 1024, l16, mkrs(ka[4], ka[5], 16 * 1568), hd * 1568 + wv * 1024);
      }
      if (kp == 0) STAMP(1);
      KST(12 + kp);
      if (kp == 7) break;
      if (kp >= 4) SSTAMP(1 + kp);
      if (kp > 0) UNR for (int h = 0; h < (kp == 6 ? 4 : 2); h++) cw = mfma(cf[h], ones, cw);
      if (kp == 6 && r == 0) *(LAS f32x4*)(L_CW + wv * 64 + q * 16) = cw;
      WAITV(n1 - needw(kp, WX) + (kp >= SP && kp < KF ? 3 : 0) - (kp == 5 ? 2 : 0));
      KST(4 + kp);
      if (kp == 0) ESTAMP(2);
      if (kp >= 4) SSTAMP(10 + kp);
      if (kp == 0 || kp == 6) BARRIER(); else SBAR();
      if (kp == 0) STAMP(3);
      if (kp == KF) {
        asm volatile("" : "+s"(tag));
        if (wv == 3 && lane < 2) gst2(__builtin_bit_cast(u32x2, tag + 1), lane ? WS_FA2 + (t1 * 16 + hd) * 8 : WS_FA1 + (hd * 16 + t1) * 8, rS, 0);
      }
    }
    }
    STAMP(4);
    gld2(fl, fo1, rS, WS_FA1 + hd * 128);
    constexpr unsigned P0 = 448;
    constexpr int NP = (576 - P0 + 39) / 40;
    const bool slow = (wwin & 127) > 64;
    if (!slow) { const unsigned n = min(P0 + (__builtin_popcount(0xD5ABu & ((1u << hd) - 1u)) * 4 + wv) * NP, 576u - NP);
      UNR for (int i = 0; i < NP; i++) gst(sent4, l16, rS, WS_HH + t1 * GS + (n + i) * 1024); }
    ESTAMP(10);
    { const unsigned L = ((i2 / 7) * 2 + (wv & 1)) * 56 + lane;
      if (wv < 2) pfl(L_PHI + 1792, lane < 56 ? L * 896 + 128 * ct : OOB, mkrs(ka[6], ka[7], spare ? 0 : 448 * 896)); }
    constexpr int NE = NX ? 16 : 10;
#define PFW1() do { if (!slow) { const unsigned n = (((ii >> 3) * 5 + __builtin_popcount(0xD5ABu & ((1u << hd) - 1u)) - 5 * (x8 & 1)) * 4 + wv) * 64 + lane; \
      if (PF1D) __builtin_amdgcn_s_sleep(PF1D); \
      UNR for (int j = 0; j < PF1; j++) { const unsigned a = n + j * 5120; pfl(L_PHI + 1792, (a < 9856 ? a + 2688 : a < 12544 ? a - 9856 : OOB >> 7) << 7, mkrs(ka[8], ka[9], 448 * 3584)); } } } while (0)
    if (PF1 && PF1P == 0) PFW1();
    ESTAMP(28);
    ESTAMP(9);
    SBAR();
    STAMP(5);
    { const unsigned ab = (4 * q + qq) * 32 + ((p ^ q) << 3), aA = ab + (wv >> 1) * 8192, aB = ab + 16384 + (wv & 1) * 512;
      f32x4 k0 = zero4, k1 = zero4;
      UNR for (int kk = 0; kk < 4; kk++) { const bf16x8 b = trfrag(aB + kk * 2048, aB + kk * 2048 + 1024);
        k0 = mfma(trfrag(aA + kk * 2048, aA + kk * 2048 + 1024), b, k0); k1 = mfma(trfrag(aA + kk * 2048 + 512, aA + kk * 2048 + 1536), b, k1); }
      gst(pk8(k0, k1), l16, rS, kvslot); }
    if (PF1 && PF1P == 1) PFW1();
    ESTAMP(6); LST(6);
    if (slow) WAITV(2); else WAITV(2 + NP + PF1);
    SBAR();
    constexpr int WL = WOS;
    { const u32x4 rWO = mkrs(ka[6], ka[7], spare ? 0 : ZWO * 448 * 896);
      if (ZWO) UNR for (int m = 0; m < WL; m++) glds(L_WO + wv * 1024 + m * 4096, v896, rWO, (wv + 4 * m) * 7168 + 128 * ct); }
    STAMP(7);
    tag += 1;
    if (slow) WAITV(1 + WL); else WAITV(1 + WL + NP + PF1);
    asm volatile("" : "+v"(fl) :: "memory");
    FLAGS(fl, fo1, WS_FA1 + hd * 128);
    u32x4 gv[16];
    const unsigned kvs = WS_KV + (hd * 64 + wv) * 1024;
    UNR for (int i = 0; i < 16; i++) { if (PL & 8) gldn<0>(gv[i], l16, rS, kvs + i * 4096); else gld<0>(gv[i], l16, rS, kvs + i * 4096); }
    { const u32x4 rWO = mkrs(ka[6], ka[7], spare ? 0 : 448 * 896);
      UNR for (int m = WOS; m < 14; m++) glds(L_WO + wv * 1024 + m * 4096, v896, rWO, (wv + 4 * m) * 7168 + 128 * ct); }
    STAMP(8);
    bf16x8 sel1, sel2;
    { const unsigned one = (r >> 2) == q ? ((r & 1) ? 0x3f800000u : 0x3f80u) : 0u, e0 = (r & 2) ? 0u : one, e1 = (r & 2) ? one : 0u;
      sel1 = asb(u32x4{e0, e1, 0u, 0u}); sel2 = asb(u32x4{0u, 0u, e0, e1}); }
    f32x4 s0 = zero4, s1 = zero4;
    UNR for (int i = 0; i < 16; i++) {
      GOT(15 - i + 14 - WOS + (i < NE ? i : NE) + (W1M ? 1 : 0), gv[i], l16, kvs + i * 4096);
      s0 = mfma(sel1, asb(gv[i]), s0); s1 = mfma(sel2, asb(gv[i]), s1);
      if (i < NE) W1P(i < 10 ? 3 + (i >> 1) : (i - 10) >> 1, i & 1);
    }
    *(LAS u32x4*)(L_KVF + wv * 1024 + l16) = pk8(s0, s1);
    STAMP(11); LSP(2);
    BARRIER();
    STAMP(12);
    WAITV(NE);
  } else {
    const unsigned c = wv - 4;
    if (lane < 2) *(LAS u32x2*)(L_ONE + lane * 256) = u32x2{ONE2, ONE2};
    const unsigned aW0 = L_W + wrow + oW0, aW1 = L_W + wrow + oW1;
    const unsigned sK = p < 3 ? 2048 : 0; unsigned aK = p < 3 ? aW1 : L_ONE;
    const unsigned aX = (4 * c + (r >> 3)) * 1024 + (r & 7) * 128 + ((q ^ (((r >> 1) & 3) << 1)) << 4);
    u32x4 xf[2][14];
    bf16x8 ph0, ph1; bf16x4 wa[8], wb[8];
    f32x4 aK0[2] = {zero4, zero4}, aK1[2] = {zero4, zero4}, aV0[2] = {zero4, zero4}, aV1[2] = {zero4, zero4}, aQ[2] = {zero4, zero4}, cw[4];
#define SB() __builtin_amdgcn_sched_barrier(0)
#define M1(c, a, b) do { if (h_ > 0) c = mfma(a, b, c); SB(); } while (0)
#define T1(w, e, a) do { if (h_ < 14) w[e] = trd(a); SB(); } while (0)
#define FR(w, e) __builtin_shufflevector(w[2 * (e)], w[2 * (e) + 1], 0, 1, 2, 3, 4, 5, 6, 7)
#define HS(w, m, h) do { const int h_ = (h), hr_ = h_ < 14 ? h_ : 0, hm_ = h_ > 0 ? h_ - 1 : 0; const unsigned o_ = h_ * 2048, ax_ = (aX ^ (64 * (h_ & 1))) + ((h_ >> 1) & 3) * 16384; \
      const bf16x8 x0_ = asb(xf[0][hm_]), x1_ = asb(xf[1][hm_]); \
      if (h_ < 14) xf[0][hr_] = ldsu4(ax_); SB(); M1(aQ[0], x0_, x0_); if (h_ < 14) xf[1][hr_] = ldsu4(ax_ + 2048); SB(); M1(aQ[1], x1_, x1_); \
      T1(w, 2, aK); M1(aK1[0], FR(m, 1), x0_); T1(w, 3, aK + 256); aK += sK; M1(aK1[1], FR(m, 1), x1_); \
      T1(w, 0, aW0 + o_); M1(aK0[0], FR(m, 0), x0_); T1(w, 1, aW0 + o_ + 256); M1(aK0[1], FR(m, 0), x1_); \
      T1(w, 4, aW0 + o_ + 28672); M1(aV0[0], FR(m, 2), x0_); T1(w, 5, aW0 + o_ + 28928); M1(aV0[1], FR(m, 2), x1_); \
      T1(w, 6, aW1 + o_ + 28672); M1(aV1[0], FR(m, 3), x0_); T1(w, 7, aW1 + o_ + 28928); M1(aV1[1], FR(m, 3), x1_); } while (0)
    UNR for (int kp = 0; kp < 7; kp++) {
      KST(4 + kp);
      BARRIER();
      KST(12 + kp);
      if (kp == 0) STAMP(3);
      if (kp == 5) {
        const unsigned a = L_PHI + (4 * q + qq) * 56, b0 = a + 8 * p, b1 = a + (p < 3 ? 32 + 8 * p : 24);
        ph0 = trfrag(b0, b0 + 896); ph1 = trfrag(b1, b1 + 896); SB();
      }
      if (kp == 6) { SSTAMP(5); CST(22); }
      if (kp == 0) CST(20);
      if (kp == 4) CST(21);
      if (kp == 4) SSTAMP(14);
      if (kp == 5) SSTAMP(15);
      HS(wa, wb, 2 * kp);
      HS(wb, wa, 2 * kp + 1);
      if (kp == 6) { UNR for (int f = 0; f < 4; f++) cw[f] = ldsf4(L_CW + f * 64 + q * 16); SB(); }
      if (kp == 4) SSTAMP(16);
      if (kp == 5) {
        u32x4 u0 = __builtin_bit_cast(u32x4, ph0), u1 = __builtin_bit_cast(u32x4, ph1);
        u0[2] = pad ? 0u : u0[2]; u0[3] = pad ? 0u : u0[3]; u1[2] = pad ? 0u : u1[2]; u1[3] = pad ? 0u : u1[3];
        ph0 = asb(u0); ph1 = asb(u1); SB();
      }
    }
    HS(wa, wb, 14);
    CST(23); STAMP(4); SSTAMP(10); KST(11);
    float nmu[2], rl[2], rla, inf = __builtin_inff();
    asm("" : "+s"(inf));
    const float wup = pad ? 0.f : 1.f;
    { float d[2], rs[2], X, Y;
      UNR for (int u = 0; u < 2; u++) { d[u] = 0.f; UNR for (int i = 0; i < 4; i++) d[u] = (4 * q + i == r) ? aQ[u][i] : d[u]; }
      spl32(sum4(d[0], pad ? aK1[0][0] : 0.f, d[1], pad ? aK1[1][0] : 0.f), X, Y);
      const float nmean = Y * (-1.f / 448.f), var = fmaxf(X * (1.f / 448.f) - nmean * nmean, 0.f), rsa = __builtin_amdgcn_rsqf(var + 1e-5f);
      rla = rsa * 1.4426950408889634f;
      spl16(nmean, nmu[0], nmu[1]); spl16(rla, rl[0], rl[1]); spl16(rsa, rs[0], rs[1]);
      const unsigned a = 2 * c * 1024 + r * 32 + ((q ^ (r >> 2)) << 3);
      f32x4 k0[2], k1[2];
      UNR for (int u = 0; u < 2; u++) { k0[u] = fma4(cw[0], nmu[u], aK0[u]); k1[u] = fma4(cw[1], nmu[u], aK1[u]); }
      UNR for (int u = 0; u < 2; u++) {
        const f32x4 v0 = fma4(cw[2], nmu[u], aV0[u]) * rs[u], v1 = fma4(cw[3], nmu[u], aV1[u]) * rs[u];
        *(LAS u32x2*)(a + u * 1024 + 16384) = u32x2{pk2(lo2(v0)), pk2(hi2(v0))};
        *(LAS u32x2*)(a + u * 1024 + 16896) = u32x2{pad ? 0x00003f80u : pk2(lo2(v1)), pad ? 0u : pk2(hi2(v1))};
      }
      u32x4 P[2], N[2];
      featmap2(k0, k1, ph0, ph1, rla, rl, wup, inf, P, N);
      UNR for (int u = 0; u < 2; u++) {
        *(LAS u32x2*)(a + u * 1024) = u32x2{P[u][0], P[u][1]}; *(LAS u32x2*)(a + u * 1024 + 512) = u32x2{P[u][2], P[u][3]};
        *(LAS u32x2*)(a + u * 1024 + 8192) = u32x2{N[u][0], N[u][1]}; *(LAS u32x2*)(a + u * 1024 + 8704) = u32x2{N[u][2], N[u][3]};
      }
    }
    ESTAMP(10); SSTAMP(11); CST(25); KST(19);
    BARRIER();
    STAMP(5);
    SBAR();
    STAMP(6);
    u32x4 phqP[2], phqN[2];
    { const unsigned a0 = L_WQ + wrow + oW0, a1 = L_WQ + wrow + oW1;
      f32x4 q0[2] = {zero4, zero4}, q1[2] = {zero4, zero4}, c0 = zero4, c1 = zero4;
      bf16x4 qf[QD + 1][4];
#define QR(s_, e) do { if ((s_) < 14) qf[(s_) % (QD + 1)][e] = trd(((e) & 2 ? a1 : a0) + (s_) * 2048 + ((e) & 1) * 256); SB(); } while (0)
#define QM(c, e, b) do { c = mfma(__builtin_shufflevector(qf[s_ % (QD + 1)][2 * (e)], qf[s_ % (QD + 1)][2 * (e) + 1], 0, 1, 2, 3, 4, 5, 6, 7), b, c); SB(); } while (0)
#define QS(ks) do { const int s_ = ks; QM(q0[0], 0, asb(xf[0][s_])); QR(s_ + QD, 0); QM(q0[1], 0, asb(xf[1][s_])); QR(s_ + QD, 1); QM(q1[0], 1, asb(xf[0][s_])); QR(s_ + QD, 2); \
        QM(q1[1], 1, asb(xf[1][s_])); QR(s_ + QD, 3); QM(c0, 0, ones); QM(c1, 1, ones); } while (0)
      SB();
      UNR for (int s = 0; s < QD; s++) { QR(s, 0); QR(s, 1); QR(s, 2); QR(s, 3); }
      QS(0); QS(1); QS(2); QS(3); QS(4); QS(5); QS(6); QS(7); QS(8); QS(9); QS(10); QS(11); QS(12); QS(13);
      ESTAMP(9);
      UNR for (int u = 0; u < 2; u++) { q0[u] = fma4(c0, nmu[u], q0[u]); q1[u] = fma4(c1, nmu[u], q1[u]); }
      featmap2(q0, q1, ph0, ph1, rla, rl, wup, inf, phqP, phqN);
      asm volatile("" : "+v"(phqP[0]), "+v"(phqN[0]), "+v"(phqP[1]), "+v"(phqN[1]));
    }
    STAMP(11);
    asm volatile("" : "+s"(tag));
    tag += 1;
    SBAR();
    STAMP(12);
    { const unsigned a = L_KVF + l16, oslot = WS_O + ((8 * t1 + 2 * c) * 16 + hd) * 1024;
      const bf16x8 f0 = asb(ldsu4(a)), f1 = asb(ldsu4(a + 1024)), f2 = asb(ldsu4(a + 2048)), f3 = asb(ldsu4(a + 3072));
      UNR for (int u = 0; u < 2; u++) {
        f32x4 o1 = mfma(f1, asb(phqP[u]), zero4), o0 = mfma(f0, asb(phqP[u]), zero4);
        o1 = mfma(f3, asb(phqN[u]), o1); o0 = mfma(f2, asb(phqN[u]), o0);
        const float rd = __builtin_amdgcn_rcpf(bcast_q3(o1[0]) + 1e-6f);
        gst(pk8(o0 * rd, o1 * (rd * wup)), l16, rS, oslot + u * 16384); }
      LST(2);
    }
  }
  SBAR();
  STAMP(13);
  if (spare) {
    const unsigned s = (ii >> 3) * 8 + wv, L = s * 64 + lane, j = (L * 9363u) >> 16, vo = s < 28 ? (224 * (j >> 5) + (j & 31)) * 896 + (L - 7 * j) * 128 : OOB;
    const u32x4 rw = mkrs(ka[12], ka[13], 1792 * 896);
    __builtin_amdgcn_s_sleep(PF2D);
    UNR for (int rho = 0; rho < 7; rho++) pfl(L_PHI + 1792, vo + rho * 32 * 896, rw);
    WAITV(0);
    SFLUSH();
    return;
  }
  unsigned aT[4], aL[4];
  { const unsigned f = ((2 * q + (qq >> 1)) & 3) << 1;
    UNR for (int T = 0; T < 4; T++) { aT[T] = (4 * q + qq) * 128 + (((2 * T + (p >> 1)) ^ f) << 4) + (p & 1) * 8; aL[T] = aT[T] ^ (kh << 6); } }
  bf16x8 wof[8][4];
#define WOF() do { const unsigned b = L_WO + kh * 28672, s = pad ? 0u : 2048u; \
    UNR for (int i = 0; i < 8; i++) UNR for (int L = 0; L < 4; L++) { const unsigned a = b + aL[L ^ ((i & 1) << 1)]; wof[i][L] = trfrag(a + i * 3584, a + s + i * 3584); } \
    UNR for (int i = 0; i < 8; i++) UNR for (int L = 0; L < 4; L++) asm volatile("" : "+v"(wof[i][L])); } while (0)
  u32x2 fl; gld2(fl, fo1, rS, WS_FA2 + g * 128);
  if (!NX) { W1R(0); W1R(1); W1R(2); }
  W1R(8); W1R(9); W1R(10); W1R(11);
  STAMP(14);
  WT(NX ? 4 : 7, fl);
  FLAGS(fl, fo1, WS_FA2 + g * 128);
  u32x4 gv[8];
  const unsigned os = WS_O + (ttp * 16 + 8 * kh) * 1024;
  UNR for (int e = 0; e < 8; e++) { if (PL & 4) gldno(gv[e], l16, rS, os, e); else gldo<false>(gv[e], l16, rS, os, e); }
  STAMP(15);
  WOF();
  BARRIER();
  STAMP(16);
  const unsigned stg = L_STG + wv * 4096;
  W1R(12); W1R(13);
  glds(stg, (lane >> 2) * 896 + (((lane & 3) ^ (lane >> 4)) << 4), rX, (128 * g + 64 * th + 16 * tq) * 896 + 128 * ct + 64 * kh);
  u32x2 b1r; gld2p(b1r, (lane >> 1) * 8, mkrs(ka[10], ka[11], 3584), 256 * mm);
  f32x4 h0, h1; u32x2 sts2;
  const unsigned exo = L_EX + (wv ^ 4) * 2048;
  f32x4 cs[4] = {zero4, zero4, zero4, zero4};
#define RW(j, MID) do { const unsigned tb = (j) * 32768 + wv * 4096; bf16x8 f[4]; \
    if ((j) < 3 || wv < 4) UNR for (int T = 0; T < 4; T++) f[T] = trfrag(tb + aT[T], tb + aT[T] + 2048); \
    MID; \
    if ((j) < 3 || wv < 4) UNR for (int T = 0; T < 4; T++) { if ((j) >= JQ && (j) < JR) *(LAS bf16x8*)(tb + T * 1024 + l16) = f[T]; cs[T] = mfma(f[T], ones, cs[T]); } } while (0)
  if (ER && ERP < 2) { RW(1, ); if (ER > 1 && ERP == 0) RW(0, ); SB(); }
  { f32x4 acc[4] = {zero4, zero4, zero4, zero4};
    UNR for (int i = 0; i < 8; i++) {
      GOTB(9 + 2 * ZW1 - i, gv[i], { __builtin_amdgcn_s_sleep(OB); UNR for (int j = i; j < 8; j++) gldo<true>(gv[j], l16, rS, os, j); });
      UNR for (int L = 3; L >= 0; L--) acc[L] = mfma(wof[i][L], asb(gv[i]), acc[L]);
    }
    STAMP(17); LSP(9);
    *(LAS f32x4*)(L_EX + wv * 2048 + l16) = acc[2]; *(LAS f32x4*)(L_EX + wv * 2048 + 1024 + l16) = acc[3];
    if (ER && ERP > 0) { SB(); if (ERP == 2) RW(1, ); if (ER > 1) RW(0, ); SB(); }
    WAITV(1);
    const unsigned ax = stg + r * 64 + (q & 1) * 8;
    h0 = acc[0] + unp4(*(LAS u32x2*)(ax + (((q >> 1) ^ (r >> 2)) << 4))); h1 = acc[1] + unp4(*(LAS u32x2*)(ax + (((2 + (q >> 1)) ^ (r >> 2)) << 4)));
    BARRIER();
    h0 += ldsf4(exo + l16); h1 += ldsf4(exo + 1024 + l16);
    gst(pk8(h0, h1), l16, rS, hslot);
    STAMP(18);
    UNR for (int jj = 0; jj < PE; jj++) gst(sent4, l16, rS, hhslot + jj * 1024);
    const f32x4 s4 = h0 + h1, q4 = h0 * h0 + h1 * h1;
    const float s = allq_sum(hadd(lo2(s4) + hi2(s4))), sq = allq_sum(hadd(lo2(q4) + hi2(q4)));
    sts2 = u32x2{f2u(s), f2u(sq)};
  }
  if (w == 0 && wv == 7 && lane == 0) gst2(__builtin_bit_cast(u32x2, tag), 0, rS, 0);
  const u32x4 rW2 = mkrs(ka[12], ka[13], 1792 * 896);
  const unsigned w2s = 224 * wv * 896 + 128 * ct;
#define W2R(rho, dst) do { UNR for (int e = 0; e < 4; e++) glds((dst) + e * 1024, v896, rW2, w2s + (32 * (rho) + 8 * e) * 896); } while (0)
#define W2C(rho, src) do { UNR for (int L = 0; L < 4; L++) w2f[rho][L] = trfrag((src) + aL[L], (src) + aL[L] + 2048); \
    UNR for (int L = 0; L < 4; L++) asm volatile("" : "+v"(w2f[rho][L])); } while (0)
  bf16x8 w2f[7][4];
  if (W2E == 0) W2R(0, stg);
  u32x4 hf[14]; u32x2 sv[4];
  const unsigned hs = WS_H + g * GS + tw * 14336;
  const unsigned sts = WS_ST + g * GS + tw * 2048, vs0 = lane * 8, vs1 = lane < 32 ? lane * 8 : OOB;
  UNR for (int j = 0; j < 4; j++) {
    if (j == 0 ? ER < 2 : j == 1 ? ER < 1 : true) RW(j, UNR for (int e = G1::pre(j); e < G1::pre(j + 1); e++) gldo<false>(hf[e], l16, rS, hs, e));
  }
  if (r == 0) UNR for (int T = 0; T < 4; T++) *(LAS f32x4*)(exo + T * 64 + q * 16) = cs[T];
  constexpr unsigned L_FL = L_EX + 1280;
  if (XF && wv == 4 && lane < 8) *(LAS volatile unsigned*)(L_FL + lane * 4) = 0u;
  if (W2E == 1) W2R(0, stg);
  STAMP(19);
  WT(5 + PE + NG0, b1r);
  if (SK < 0) { WAITV(4 + NG0); gst2(sts2, vst, rS, stslot); }
  BARRIER();
  STAMP(20);
  const unsigned aF = l16 + 57344;
  unsigned aU[4];
  UNR for (int T = 0; T < 4; T++) { aU[T] = aT[T] + 57344; if (JR < 4) asm volatile("" : "+v"(aU[T])); }
  { const unsigned a = L_EX + (lane >> 5) * 2048 + ((lane >> 1) & 15) * 16;
    const f32x4 c = (ldsf4(a) + ldsf4(a + 4096)) + (ldsf4(a + 8192) + ldsf4(a + 12288));
    *(LAS f32x4*)(exo + 256 + l16) = (lane & 1) ? unp4(b1r) : c; }
  TSTAMP(2);
  f32x4 acc[8]; u32x4 dmy[14];
  float rsc, nmv;
  UNR for (int L = 0; L < 8; L++) acc[L] = zero4;
#define SCALES(n) do { asm volatile("s_waitcnt vmcnt(%4)" : "+v"(sv[0]), "+v"(sv[1]), "+v"(sv[2]), "+v"(sv[3]) : "i"(n) : "memory"); \
    if (UNLIKELY(bad8(sv))) { do gld2_again4(sv, vs0, vs1, rS, sts); while (bad8(sv) && ++spins < SPIN_CAP); } \
    float a = (u2f(sv[0].x) + u2f(sv[1].x)) + (u2f(sv[2].x) + u2f(sv[3].x)), b = (u2f(sv[0].y) + u2f(sv[1].y)) + (u2f(sv[2].y) + u2f(sv[3].y)), c, d; \
    sw32(a, b); spl16(a + b, c, d); spl32(c + d, a, b); \
    const float m = a * (1.f / 448.f), var = fmaxf(b * (1.f / 448.f) - m * m, 0.f), r1 = __builtin_amdgcn_rsqf(var + 1e-5f); \
    rsc = r1 * __builtin_amdgcn_rsqf(var * r1 * r1 + 1e-5f); nmv = -m * rsc; } while (0)
#define W2STEP(ks) UNR for (int c = 0; c < NC; c++) if (ks == G1::CK[c]) { WAITV(G1::mid(ks) - G1::iw(c) - 4); W2C(c, stg); W2R(c + 1, stg); }
#define G1S(ks) { bf16x8 af[8]; \
      UNR for (int L = 0; L < 8; L++) { if (ks >= 4 * JR || ks < 4 * JQ) { const unsigned a_ = (ks < 7 ? aT[L & 3] : aU[L & 3]) + (ks % 7) * 8192 + (L >> 2) * 4096; af[L] = trfrag(a_, a_ + 2048); } \
        else af[L] = asb(ldsu4((ks < 7 ? l16 : aF) + (ks % 7) * 8192 + L * 1024)); } \
      GOT(G1::cnt(ks) - G1::ih(ks) - 1, hf[ks], l16, hs + ks * 1024); \
      if (AB2 && ks >= NG0) asm volatile("" :: "v"(dmy[ks])); \
      if (ks == SC) SCALES(G1::cnt(ks) - G1::is() - 1); \
      if (ks == 0) { LST(1); TSTAMP(6); } \
      if (ks == 13) LST(7); \
      UNR for (int L = 0; L < 8; L++) acc[L] = mfma(af[L], asb(hf[ks]), acc[L]); \
      UNR for (int e = G1::got(ks); e < G1::got(ks + 1); e++) { if (AB2) gldo<false>(dmy[e], l16, rS, WS_H + g * GS + (tw ^ 1) * 14336, e); gldo<false>(hf[e], l16, rS, hs, e); } \
      UNR for (int jj = G1::poi(ks); jj < G1::poi(ks + 1); jj++) gst(sent4, l16, rS, hhslot + jj * 1024); \
      if (ks == SK) { WAITV(G1::cnt(ks) + G1::got(ks + 1) - G1::got(ks) - G1::ip() - 1); gst2(sts2, vst, rS, stslot); } \
      if (ks == SL) { gld2o<0>(sv[0], vs0, rS, sts); gld2o<512>(sv[1], vs0, rS, sts); gld2o<1024>(sv[2], vs0, rS, sts); gld2o<1536>(sv[3], vs1, rS, sts); } \
      W2STEP(ks); \
      if (ks == 3) TSTAMP(9); \
      if (ks == 7) TSTAMP(10); \
      if (ks == 10) TSTAMP(28); }
  G1S(0) G1S(1) G1S(2) G1S(3) G1S(4) G1S(5) G1S(6) G1S(7) G1S(8) G1S(9) G1S(10) G1S(11) G1S(12) G1S(13)
  STAMP(21);
  if (SC > 13) SCALES(G1::cnt(14) - G1::is() - 1);
  LSP(10);
  if (XF && lane == 0) *(LAS volatile unsigned*)(L_FL + wv * 4) = SENT;
  if (XP0 && wv >= 4) __builtin_amdgcn_s_setprio(XP0);
  WAITV(G1::cnt(14) - G1::iw(NC) - 4);
  W2C(NC, stg); W2R(NC + 1, stg);
  u32x4 b2r; gld4p(b2r, (lane & 7) * 16, mkrs(ka[14], ka[15], 896), 128 * ct);
  STAMP(22);
#define W2REST() do { UNR for (int c = NC + 2; c < 7; c++) W2R(c, (6 - c) * 32768 + wv * 4096); } while (0)
#define XSYNC() do { if (XF) { unsigned f_; do f_ = *(LAS volatile unsigned*)(L_FL + (lane & 7) * 4); while (__builtin_amdgcn_ballot_w64(f_ != SENT) != 0 && ++spins < SPIN_CAP); } else BARRIER(); } while (0)
  if (wv >= 4) { XSYNC(); if (XP) __builtin_amdgcn_s_setprio(XP); W2REST(); }
  { UNR for (int jj = 0; jj < 4; jj++) { u32x4 gq;
      UNR for (int e = 0; e < 2; e++) { const unsigned a = exo + 256 + (2 * jj + e) * 128 + q * 32;
        const f32x4 v = fma4(acc[2 * jj + e], rsc, fma4(ldsf4(a), nmv, ldsf4(a + 16))); gq[2 * e] = gelu2(lo2(v)); gq[2 * e + 1] = gelu2(hi2(v)); }
      gst(gq, l16, rS, hhslot + jj * 1024);
      if (jj + 1 == XJ && XJ < 4 && wv < 4) XSYNC();
    } }
  STAMP(23);
  if (wv < 4) { if (XJ == 4) XSYNC(); W2REST(); }
  if (XP && XP != XPE && wv >= 4) __builtin_amdgcn_s_setprio(XPE);
  STAMP(24);
  u32x4 gh[D2];
  const unsigned hhs = WS_HH + g * GS + (4 * th * 56 + 7 * wv) * 1024;
#define TT(tp) (TM ? ((tp) & 2) | (((tp) ^ tq) & 1) : (tp) ^ tq)
#define HH1(n) gldo<false>(gh[(n) % D2], l16, rS, hhs + TT((n) / 7) * 57344, (n) % 7)
  UNR for (int n = 0; n < D2; n++) HH1(n);
  STAMP(25);
  if (wv < 4) WAITV(D2); else WAITV(D2 + 4);
  asm volatile("" : "+v"(b2r));
  W2C(NC + 1, stg);
  UNR for (int c = NC + 2; c < 7; c++) W2C(c, (6 - c) * 32768 + wv * 4096);
  STAMP(26);
  UNR for (int tp = 0; tp < 4; tp++) {
    const bool hh = TM ? (tp & 1) == 0 && (tq & 2) == tp : tp == 0;
    f32x4 a2[4] = {hh ? h0 : zero4, hh ? h1 : zero4, zero4, zero4};
    const unsigned t = TT(tp), so = hhs + t * 57344;
    UNR for (int i = 0; i < 7; i++) {
      const int n = 7 * tp + i;
      GOT(27 - n < D2 - 1 ? 27 - n : D2 - 1, gh[n % D2], l16, so + i * 1024);
      if (tp == 0 && i == 0) { LST(14); GSTAMP(2); }
      if (tp == 3 && i == 0) GSTAMP(28);
      UNR for (int L = 0; L < 4; L++) a2[L] = mfma(w2f[i][L], asb(gh[n % D2]), a2[L]);
      if (n + D2 < 28) HH1(n + D2);
    }
    if (tp == 0) GSTAMP(6);
    if (tp == 1) GSTAMP(9);
    if (tp == 2) GSTAMP(10);
    const unsigned ck = wv * 4096 + t * 32768 + (t == 3 ? 16384 : 0) + r * 256;
    UNR for (int L = 0; L < 4; L++) *(LAS f32x4*)(ck + (((4 * L + q) ^ (8 * kh) ^ r) << 4)) = a2[L];
  }
  STAMP(27); LSP(28);
  BARRIER();
  { const unsigned t = wv >> 1, tok = 8 * (wv & 1) + (lane >> 3), c8 = lane & 7;
    const unsigned b = t * 32768 + (t == 3 ? 16384 : 0) + tok * 256, e0 = b + (((2 * c8) ^ tok) << 4), e1 = b + (((2 * c8 + 1) ^ tok) << 4);
    f32x4 s0 = unp4(u32x2{b2r[0], b2r[1]}), s1 = unp4(u32x2{b2r[2], b2r[3]});
    UNR for (int v = 0; v < 8; v++) { s0 += ldsf4(e0 + v * 4096); s1 += ldsf4(e1 + v * 4096); }
    gstp(pk8(s0, s1), (lane >> 3) * 896 + c8 * 16, mkrs(kb4[0], kb4[1], 2048 * 896), (128 * g + 64 * th + 8 * wv) * 896 + 128 * ct);
  }
  asm volatile("fk_to:");
  STAMP(NSTAMP - 1); CSTAMP(31);
  SFLUSH();
}

extern "C" void kernel_launch(void* const* d_in, const int* in_sizes, int n_in, void* d_out, int out_size, void* d_ws, size_t ws_size, hipStream_t stream) {
  (void)hipFuncSetAttribute((const void*)fused_kernel, hipFuncAttributeMaxDynamicSharedMemorySize, LDS_BYTES);
  fused_kernel<<<256, 512, LDS_BYTES, stream>>>(d_in[0], d_in[1], d_in[2], d_in[3], d_in[5], d_in[6], d_in[7], d_in[8], d_out, d_ws);
}
```
